```python
import torch

def _softmax(x, axis=-1):
    return torch.softmax(x, dim=axis)
def _gelu(x):
    return torch.nn.functional.gelu(x, approximate="tanh")
def _silu(x):
    return torch.nn.functional.silu(x)
def _sigmoid(x):
    return torch.sigmoid(x)
def _layernorm(x, axis=-1, eps=1e-5):
    m = x.mean(dim=axis, keepdim=True)
    v = x.var(dim=axis, keepdim=True, unbiased=False)
    return (x - m) / torch.sqrt(v + eps)
def _scatter_add(tgt, idx, src, axis=0):
    return tgt.clone().index_add_(axis, idx, src)
def _segment_sum(src, seg, n):
    out = torch.zeros((n,) + src.shape[1:], dtype=src.dtype)
    return out.index_add_(0, seg, src)
def _pad(x, pw):
    flat = tuple(v for pair in reversed(pw) for v in pair)
    return torch.nn.functional.pad(x, flat)

kernel_name = 'recurrent_scan__spectral_fnet__mixer_channel_mlp__prompt'

def setup_inputs():

    _w = (lambda a: (lambda t: t.to(torch.bfloat16) if t.is_floating_point() else t)(torch.as_tensor(a)))
    def _A_setup():
        B, S, D = 1, 2048, 576
        ns = torch.arange(S)[:, None] * torch.arange(S)[None, :]
        nd = torch.arange(D)[:, None] * torch.arange(D)[None, :]
        Ws_re = torch.cos(-2.0 * torch.pi * ns / S).to(float)
        Ws_im = torch.sin(-2.0 * torch.pi * ns / S).to(float)
        Wd_re = torch.cos(-2.0 * torch.pi * nd / D).to(float)
        Wd_im = torch.sin(-2.0 * torch.pi * nd / D).to(float)
        return {
            "x": _w((lambda *s: torch.randn(*s, dtype=torch.bfloat16) * 0.05)(B, S, D)),
            "dft_seq_re": _w(Ws_re),
            "dft_seq_im": _w(Ws_im),
            "dft_hid_re": _w(Wd_re),
            "dft_hid_im": _w(Wd_im),
        }
    _A_in = _A_setup()

    def _B_setup():
        B, S, D, F = 1, 2048, 576, 4608
        return {
            "x": _w((lambda *s: torch.randn(*s, dtype=torch.bfloat16) * 0.05)(B, S, D)),
            "w1": _w((lambda *s: torch.randn(*s, dtype=torch.bfloat16) * 0.8)(D, F) * (D ** -0.5)),
            "b1": _w((lambda *s: torch.randn(*s, dtype=torch.bfloat16) * 0.8)(F)),
            "w2": _w((lambda *s: torch.randn(*s, dtype=torch.bfloat16) * 0.05)(F, D) * (F ** -0.5)),
            "b2": _w((lambda *s: torch.randn(*s, dtype=torch.bfloat16) * 0.05)(D)),
        }
    _B_in = _B_setup()

    return {
        "A_x": _A_in["x"],
        "A_dft_seq_re": _A_in["dft_seq_re"],
        "A_dft_seq_im": _A_in["dft_seq_im"],
        "A_dft_hid_re": _A_in["dft_hid_re"],
        "A_dft_hid_im": _A_in["dft_hid_im"],
        "B_x": _B_in["x"],
        "B_w1": _B_in["w1"],
        "B_b1": _B_in["b1"],
        "B_w2": _B_in["w2"],
        "B_b2": _B_in["b2"],
    }

def reference(A_x, A_dft_seq_re, A_dft_seq_im, A_dft_hid_re, A_dft_hid_im, B_x, B_w1, B_b1, B_w2, B_b2):
    def _A_ref(x, dft_seq_re, dft_seq_im, dft_hid_re, dft_hid_im):
        a = torch.einsum("bsd,ed->bse", x, dft_hid_re)
        b = torch.einsum("bsd,ed->bse", x, dft_hid_im)
        y = torch.einsum("ts,bse->bte", dft_seq_re, a) - torch.einsum("ts,bse->bte", dft_seq_im, b)
        return y

    def _B_ref(x, w1, b1, w2, b2):
        y = _layernorm(x, axis=-1)
        h = _gelu(torch.einsum("bsd,df->bsf", y, w1) + b1)
        return torch.einsum("bsf,fd->bsd", h, w2) + b2

    x = A_x
    xn = _layernorm(x, axis=-1)
    s = _A_ref(xn, A_dft_seq_re, A_dft_seq_im, A_dft_hid_re, A_dft_hid_im)
    g = _silu(_B_ref(xn, B_w1, B_b1, B_w2, B_b2))
    return x + s * g
```

```cpp
#include <hip/hip_runtime.h>
#include <hip/hip_bf16.h>
#include <stdint.h>


typedef unsigned short u16;
typedef short bf16x8 __attribute__((ext_vector_type(8)));
typedef short s16x4 __attribute__((ext_vector_type(4)));
typedef float f32x4 __attribute__((ext_vector_type(4)));
typedef unsigned u32x4 __attribute__((ext_vector_type(4)));
typedef unsigned u32x2 __attribute__((ext_vector_type(2)));
#define LAS __attribute__((address_space(3)))
#define GAS __attribute__((address_space(1)))
#define DI __device__ __forceinline__

constexpr int S_ = 2048, D_ = 576, F_ = 4608;

DI float bf2f(unsigned b) { return __uint_as_float(b << 16); }
typedef __bf16 bf16x2v __attribute__((ext_vector_type(2)));
typedef float f32x2 __attribute__((ext_vector_type(2)));
DI unsigned pk_bf16(float lo, float hi) {
  bf16x2v r = __builtin_convertvector((f32x2){lo, hi}, bf16x2v);
  return __builtin_bit_cast(unsigned, r);
}
DI float rbf(float x) { return __uint_as_float(pk_bf16(x, x) << 16); }
DI float lo_f(unsigned p) { return __uint_as_float(p << 16); }
DI float hi_f(unsigned p) { return __uint_as_float(p & 0xffff0000u); }

template <int N> DI void wait_vm() { asm volatile("s_waitcnt vmcnt(%0)" ::"n"(N) : "memory"); }
DI void raw_barrier() {
  asm volatile("s_waitcnt lgkmcnt(0)" ::: "memory");
  __builtin_amdgcn_s_barrier();
  asm volatile("" ::: "memory");
}

DI float wave_sum(float v) {
#pragma unroll
  for (int o = 32; o > 0; o >>= 1) v += __shfl_xor(v, o);
  return v;
}

DI void ln_row(float (&v)[16], bool has2) {
  float s = 0.f;
#pragma unroll
  for (int i = 0; i < 8; ++i) s += v[i];
  if (has2) {
#pragma unroll
    for (int i = 8; i < 16; ++i) s += v[i];
  }
  s = wave_sum(s);
  const float mean = s * (1.0f / D_);
  float q = 0.f;
#pragma unroll
  for (int i = 0; i < 8; ++i) { float d = v[i] - mean; q += d * d; }
  if (has2) {
#pragma unroll
    for (int i = 8; i < 16; ++i) { float d = v[i] - mean; q += d * d; }
  }
  q = wave_sum(q);
  const float var = rbf(q * (1.0f / D_));
  const float mb = rbf(mean);
  const float den = rbf(sqrtf(rbf(var + 1e-5f)));
#pragma unroll
  for (int i = 0; i < 16; ++i) v[i] = rbf(rbf(v[i] - mb) / den);
}

__global__ __launch_bounds__(256) void k_ln(const u16* __restrict__ x, u16* __restrict__ xn, u16* __restrict__ y) {
  const int lane = threadIdx.x & 63;
  const int row = blockIdx.x * 4 + (threadIdx.x >> 6);
  const bool has2 = lane < 8;
  const u16* xr = x + (size_t)row * D_;
  u32x4 p0 = *(const u32x4*)(xr + lane * 8);
  u32x4 p1 = {0u, 0u, 0u, 0u};
  if (has2) p1 = *(const u32x4*)(xr + 512 + lane * 8);
  float v[16];
#pragma unroll
  for (int i = 0; i < 4; ++i) { v[2 * i] = lo_f(p0[i]); v[2 * i + 1] = hi_f(p0[i]); v[8 + 2 * i] = lo_f(p1[i]); v[9 + 2 * i] = hi_f(p1[i]); }
  ln_row(v, has2);
#pragma unroll
  for (int i = 0; i < 4; ++i) { p0[i] = pk_bf16(v[2 * i], v[2 * i + 1]); p1[i] = pk_bf16(v[8 + 2 * i], v[9 + 2 * i]); }
  *(u32x4*)(xn + (size_t)row * D_ + lane * 8) = p0;
  if (has2) *(u32x4*)(xn + (size_t)row * D_ + 512 + lane * 8) = p1;
  ln_row(v, has2);
#pragma unroll
  for (int i = 0; i < 4; ++i) { p0[i] = pk_bf16(v[2 * i], v[2 * i + 1]); p1[i] = pk_bf16(v[8 + 2 * i], v[9 + 2 * i]); }
  *(u32x4*)(y + (size_t)row * D_ + lane * 8) = p0;
  if (has2) *(u32x4*)(y + (size_t)row * D_ + 512 + lane * 8) = p1;
}

template <int BK> DI int kswz(int r) {
  if (BK == 64) return (r >> 1) & 7;
  else { int i = (r >> 2) & 3; return i ^ ((i & 1) << 1); }
}

template <int BM, int BN, int BK, int WM, int WN, int NS, bool BNN, bool SWAP>
struct Gemm {
  static constexpr int NW = WM * WN, TM = BM / WM, TN = BN / WN, MI = TM / 16, NI = TN / 16, KS = BK / 32;
  static constexpr int RB = BK * 2;
  static constexpr int A_BYTES = BM * RB, B_BYTES = BN * RB, STAGE = A_BYTES + B_BYTES;
  static constexpr int AI = A_BYTES / 1024 / NW;
  static constexpr int BTOT = B_BYTES / 1024, BI = BTOT / NW, BX = BTOT % NW;
  static constexpr int CNT = AI + BI;
  static constexpr int LDS_BYTES = NS * STAGE;
  static_assert(A_BYTES % (1024 * NW) == 0, "A split");
  static_assert(B_BYTES % 1024 == 0, "B split");

  unsigned voffA[AI], voffB[BI + 1];
  unsigned aoff[KS], boff[(BNN && BN != 144) ? NI : KS];
  int wave;

  static DI int nn_pos(int k) {
    if (BN == 144) return (k & ~12) | ((k & 4) << 1) | ((k & 8) >> 1);
    else return k;
  }
  static DI int nn_cx(int k) {
    if (BN == 144) return 0;
    else return (((k >> 1) & 1) | (((k >> 3) & 1) << 1)) << 1;
  }

  DI void init(int lda_b, int ldb_b) {
    const int tid = threadIdx.x, lane = tid & 63;
    wave = __builtin_amdgcn_readfirstlane(tid >> 6);
    const int wm = wave / WN, wn = wave % WN;
    constexpr int CPR = RB / 16;
#pragma unroll
    for (int i = 0; i < AI; ++i) {
      int b = (wave * AI + i) * 1024 + lane * 16;
      int r = b / RB, c = (b % RB) / 16;
      voffA[i] = (unsigned)(r * lda_b + ((c ^ kswz<BK>(r)) * 16));
    }
#pragma unroll
    for (int i = 0; i < BI + 1; ++i) {
      int j = (i < BI) ? (wave * BI + i) : (NW * BI + wave);
      if (j >= BTOT) j = BTOT - 1;
      int b = j * 1024 + lane * 16;
      if (!BNN) {
        int r = b / RB, c = (b % RB) / 16;
        voffB[i] = (unsigned)(r * ldb_b + ((c ^ kswz<BK>(r)) * 16));
      } else {
        int p = b / (BN * 2), c = (b % (BN * 2)) / 16;
        int k = nn_pos(p);
        voffB[i] = (unsigned)(k * ldb_b + ((c ^ nn_cx(k)) * 16));
      }
    }
    (void)CPR;
    const int fr = lane & 15, fq = lane >> 4;
#pragma unroll
    for (int kk = 0; kk < KS; ++kk) {
      int r = wm * TM + fr;
      aoff[kk] = (unsigned)(r * RB + (((kk * 4 + fq) ^ kswz<BK>(r)) * 16));
    }
    if (!BNN) {
#pragma unroll
      for (int kk = 0; kk < KS; ++kk) {
        int r = wn * TN + fr;
        boff[kk] = (unsigned)(A_BYTES + r * RB + (((kk * 4 + fq) ^ kswz<BK>(r)) * 16));
      }
    } else {
      const int g = lane >> 4, q = (lane & 15) >> 2, p = lane & 3;
      const int k = 8 * g + q;
      if (BN == 144) {
        boff[0] = (unsigned)(A_BYTES + nn_pos(k) * (BN * 2) + (wn * TN / 8 + (p >> 1)) * 16 + (p & 1) * 8);
      } else {
        const int u = nn_cx(k) >> 1;
#pragma unroll
        for (int j = 0; j < NI; ++j)
          boff[j] = (unsigned)(A_BYTES + k * (BN * 2) + (((((wn * NI + j) ^ u) << 1) + (p >> 1)) * 16) + (p & 1) * 8);
      }
    }
  }

  DI void stage(LAS char* buf, const char* gA, const char* gB) const {
#pragma unroll
    for (int i = 0; i < AI; ++i)
      __builtin_amdgcn_global_load_lds((const GAS unsigned*)(gA + voffA[i]), (LAS unsigned*)(buf + (wave * AI + i) * 1024), 16, 0, 0);
#pragma unroll
    for (int i = 0; i < BI; ++i)
      __builtin_amdgcn_global_load_lds((const GAS unsigned*)(gB + voffB[i]), (LAS unsigned*)(buf + A_BYTES + (wave * BI + i) * 1024), 16, 0, 0);
    if (BX > 0) {
      if (wave < BX)
        __builtin_amdgcn_global_load_lds((const GAS unsigned*)(gB + voffB[BI]), (LAS unsigned*)(buf + A_BYTES + (NW * BI + wave) * 1024), 16, 0, 0);
    }
  }
  template <int R> DI void wait_stages() const {
    if (BX > 0) {
      if (wave < BX) wait_vm<R*(CNT + 1)>(); else wait_vm<R * CNT>();
    } else wait_vm<R * CNT>();
  }

  DI void load_frags(const LAS char* buf, int kk, bf16x8 (&a)[MI], bf16x8 (&b)[NI]) const {
#pragma unroll
    for (int mi = 0; mi < MI; ++mi) a[mi] = *(const LAS bf16x8*)(buf + aoff[kk] + mi * 16 * RB);
    if (!BNN) {
#pragma unroll
      for (int ni = 0; ni < NI; ++ni) b[ni] = *(const LAS bf16x8*)(buf + boff[kk] + ni * 16 * RB);
    } else {
#pragma unroll
      for (int ni = 0; ni < NI; ++ni) {
        s16x4 lo, hi;
        if (BN == 144) {
          const LAS char* p = buf + boff[0] + kk * 32 * (BN * 2) + ni * 32;
          lo = __builtin_amdgcn_ds_read_tr16_b64_v4i16((LAS s16x4*)(p));
          hi = __builtin_amdgcn_ds_read_tr16_b64_v4i16((LAS s16x4*)(p + 8 * (BN * 2)));
        } else {
          const LAS char* p = buf + boff[ni] + kk * 32 * (BN * 2);
          lo = __builtin_amdgcn_ds_read_tr16_b64_v4i16((LAS s16x4*)(p));
          hi = __builtin_amdgcn_ds_read_tr16_b64_v4i16((LAS s16x4*)(p + 4 * (BN * 2)));
        }
        b[ni] = __builtin_shufflevector(lo, hi, 0, 1, 2, 3, 4, 5, 6, 7);
      }
    }
  }

  DI void mma(const bf16x8 (&a)[MI], const bf16x8 (&b)[NI], f32x4 (&acc)[MI][NI]) const {
#pragma unroll
    for (int mi = 0; mi < MI; ++mi)
#pragma unroll
      for (int ni = 0; ni < NI; ++ni) {
        if (SWAP) acc[mi][ni] = __builtin_amdgcn_mfma_f32_16x16x32_bf16(b[ni], a[mi], acc[mi][ni], 0, 0, 0);
        else acc[mi][ni] = __builtin_amdgcn_mfma_f32_16x16x32_bf16(a[mi], b[ni], acc[mi][ni], 0, 0, 0);
      }
  }

  template <int NK>
  DI void run(LAS char* smem, const char* gA, const char* gB, long astep, long bstep, f32x4 (&acc)[MI][NI]) const {
    static_assert(NK >= NS, "NK");
    bf16x8 a0[MI], b0[NI], a1[MI], b1[NI];
#pragma unroll
    for (int s = 0; s < NS - 1; ++s) stage(smem + s * STAGE, gA + s * astep, gB + s * bstep);
    wait_stages<NS - 2>();
    raw_barrier();
    load_frags(smem, 0, a0, b0);
    int cur = 0;
    auto body = [&](int k, auto tail_c) {
      constexpr int TAIL = decltype(tail_c)::value;
      LAS char* bufc = smem + cur * STAGE;
      int nxt = cur + 1; if (nxt == NS) nxt = 0;
      LAS char* bufn = smem + nxt * STAGE;
      if (KS == 2) {
        load_frags(bufc, 1, a1, b1);
        mma(a0, b0, acc);
      }
      if (TAIL != 0) {
        if (TAIL < 0) wait_stages<NS - 3>(); else wait_stages<(TAIL - 1 < NS - 3 ? TAIL - 1 : NS - 3)>();
        raw_barrier();
        if (TAIL < 0) {
          int prv = cur - 1; if (prv < 0) prv = NS - 1;
          stage(smem + prv * STAGE, gA + (long)(k + NS - 1) * astep, gB + (long)(k + NS - 1) * bstep);
        }
        if (KS == 2) {
          load_frags(bufn, 0, a0, b0);
          mma(a1, b1, acc);
        } else {
          load_frags(bufn, 0, a1, b1);
          mma(a0, b0, acc);
#pragma unroll
          for (int mi = 0; mi < MI; ++mi) a0[mi] = a1[mi];
#pragma unroll
          for (int ni = 0; ni < NI; ++ni) b0[ni] = b1[ni];
        }
      } else {
        if (KS == 2) mma(a1, b1, acc); else mma(a0, b0, acc);
      }
      cur = nxt;
    };
    for (int k = 0; k < NK - NS + 1; ++k) body(k, std::integral_constant<int, -1>{});
    if constexpr (NS - 2 >= 4) body(NK - 5, std::integral_constant<int, 4>{});
    if constexpr (NS - 2 >= 3) body(NK - 4, std::integral_constant<int, 3>{});
    if constexpr (NS - 2 >= 2) body(NK - 3, std::integral_constant<int, 2>{});
    if constexpr (NS - 2 >= 1) body(NK - 2, std::integral_constant<int, 1>{});
    body(NK - 1, std::integral_constant<int, 0>{});
  }
};

DI float gelu_f(float x) {
  const float c0 = -2.0f * 0.7978845608028654f * 1.4426950408889634f;
  const float c1 = c0 * 0.044715f;
  float z = x * __builtin_fmaf(x * x, c1, c0);
  float e = __builtin_amdgcn_exp2f(z);
  return x * __builtin_amdgcn_rcpf(1.0f + e);
}

constexpr int K2_BK = 32, K2_NS = 5;
using G2a = Gemm<256, 192, K2_BK, 4, 2, K2_NS, false, false>;
using G2h = Gemm<256, 192, K2_BK, 4, 2, K2_NS, true, true>;

__global__ __launch_bounds__(512, 2) void k_gemm1(const u16* __restrict__ xn, const u16* __restrict__ y,
                                                  const u16* __restrict__ wd_re, const u16* __restrict__ wd_im,
                                                  const u16* __restrict__ w1, const u16* __restrict__ b1,
                                                  u16* __restrict__ abT, u16* __restrict__ h) {
  __shared__ __attribute__((aligned(1024))) char smem_[G2a::LDS_BYTES];
  LAS char* smem = (LAS char*)smem_;
  const int b = blockIdx.x;
  const int t = (b & 7) * 30 + (b >> 3);
  const int pm = t & 7, pn = t >> 3;
  const int lane = threadIdx.x & 63, fr = lane & 15, fq = lane >> 4;
  f32x4 acc[4][6];
#pragma unroll
  for (int i = 0; i < 4; ++i)
#pragma unroll
    for (int j = 0; j < 6; ++j) acc[i][j] = (f32x4){0.f, 0.f, 0.f, 0.f};
  if (pn < 6) {
    G2a g;
    g.init(D_ * 2, D_ * 2);
    const u16* wd = pn < 3 ? wd_re : wd_im;
    const int n0 = (pn < 3 ? pn : pn - 3) * 192;
    const char* gA = (const char*)(xn + (size_t)pm * 256 * D_);
    const char* gB = (const char*)(wd + (size_t)n0 * D_);
    g.run<D_ / K2_BK>(smem, gA, gB, K2_BK * 2, K2_BK * 2, acc);
    const int wm = g.wave / 2, wn = g.wave % 2;
    u16* dst = abT + (size_t)(pn < 3 ? 0 : 1) * D_ * S_;
#pragma unroll
    for (int ni = 0; ni < 6; ++ni) {
      const int e = n0 + wn * 96 + ni * 16 + fr;
#pragma unroll
      for (int mp = 0; mp < 2; ++mp) {
        unsigned x0 = pk_bf16(acc[2 * mp][ni][0], acc[2 * mp][ni][1]), x1 = pk_bf16(acc[2 * mp][ni][2], acc[2 * mp][ni][3]);
        unsigned y0 = pk_bf16(acc[2 * mp + 1][ni][0], acc[2 * mp + 1][ni][1]), y1 = pk_bf16(acc[2 * mp + 1][ni][2], acc[2 * mp + 1][ni][3]);
        auto r0 = __builtin_amdgcn_permlane16_swap(x0, y0, false, false);
        auto r1 = __builtin_amdgcn_permlane16_swap(x1, y1, false, false);
        u32x4 w = {r0[0], r1[0], r0[1], r1[1]};
        const int s = pm * 256 + wm * 64 + (2 * mp + (fq & 1)) * 16 + (fq >> 1) * 8;
        *(u32x4*)(dst + (size_t)e * S_ + s) = w;
      }
    }
  } else {
    G2h g;
    g.init(D_ * 2, F_ * 2);
    const int n0 = (pn - 6) * 192;
    const char* gA = (const char*)(y + (size_t)pm * 256 * D_);
    const char* gB = (const char*)(w1 + n0);
    g.run<D_ / K2_BK>(smem, gA, gB, K2_BK * 2, (long)K2_BK * F_ * 2, acc);
    const int wm = g.wave / 2, wn = g.wave % 2;
    float bias[6][4];
#pragma unroll
    for (int ni = 0; ni < 6; ++ni) {
      u32x2 p = *(const u32x2*)(b1 + n0 + wn * 96 + ni * 16 + 4 * fq);
      bias[ni][0] = lo_f(p[0]); bias[ni][1] = hi_f(p[0]); bias[ni][2] = lo_f(p[1]); bias[ni][3] = hi_f(p[1]);
    }
#pragma unroll
    for (int mi = 0; mi < 4; ++mi) {
      const int m = pm * 256 + wm * 64 + mi * 16 + fr;
#pragma unroll
      for (int np = 0; np < 3; ++np) {
        unsigned pk[2][2];
#pragma unroll
        for (int o = 0; o < 2; ++o) {
          const int ni = 2 * np + o;
          float v[4];
#pragma unroll
          for (int j = 0; j < 4; ++j) v[j] = gelu_f(rbf(rbf(acc[mi][ni][j]) + bias[ni][j]));
          pk[o][0] = pk_bf16(v[0], v[1]); pk[o][1] = pk_bf16(v[2], v[3]);
        }
        auto r0 = __builtin_amdgcn_permlane16_swap(pk[0][0], pk[1][0], false, false);
        auto r1 = __builtin_amdgcn_permlane16_swap(pk[0][1], pk[1][1], false, false);
        u32x4 w = {r0[0], r1[0], r0[1], r1[1]};
        const int f = n0 + wn * 96 + (2 * np + (fq & 1)) * 16 + (fq >> 1) * 8;
        *(u32x4*)(h + (size_t)m * F_ + f) = w;
      }
    }
  }
}

constexpr int K3_NS = 4;
using G3s = Gemm<128, 144, 64, 4, 1, K3_NS, false, true>;
using G3o = Gemm<128, 144, 64, 4, 1, K3_NS, true, true>;

__global__ __launch_bounds__(256, 1) void k_gemm2(const u16* __restrict__ ws_re, const u16* __restrict__ ws_im,
                                                  const u16* __restrict__ abT, const u16* __restrict__ h,
                                                  const u16* __restrict__ w2, float* __restrict__ part) {
  __shared__ __attribute__((aligned(1024))) char smem_[G3s::LDS_BYTES];
  LAS char* smem = (LAS char*)smem_;
  const int b = blockIdx.x;
  const int xcd = b & 7, j = b >> 3;
  const int ks = xcd >> 1, pm = (xcd & 1) * 8 + (j >> 2), pn = j & 3;
  const int lane = threadIdx.x & 63;
  f32x4 acc[2][9];
#pragma unroll
  for (int i = 0; i < 2; ++i)
#pragma unroll
    for (int jj = 0; jj < 9; ++jj) acc[i][jj] = (f32x4){0.f, 0.f, 0.f, 0.f};
  int wave;
  if (ks < 2) {
    G3s g;
    g.init(S_ * 2, S_ * 2);
    wave = g.wave;
    const char* gA = (const char*)((ks == 0 ? ws_re : ws_im) + (size_t)pm * 128 * S_);
    const char* gB = (const char*)(abT + (size_t)ks * D_ * S_ + (size_t)pn * 144 * S_);
    g.run<S_ / 64>(smem, gA, gB, 128, 128, acc);
  } else {
    G3o g;
    g.init(F_ * 2, D_ * 2);
    wave = g.wave;
    const int k0 = (ks - 2) * (F_ / 2);
    const char* gA = (const char*)(h + (size_t)pm * 128 * F_ + k0);
    const char* gB = (const char*)(w2 + (size_t)k0 * D_ + pn * 144);
    g.run<F_ / 2 / 64>(smem, gA, gB, 128, (long)64 * D_ * 2, acc);
  }
  float* dst = part + ((size_t)(ks * 64 + pm * 4 + pn) * 4 + wave) * (18 * 256) + lane * 4;
#pragma unroll
  for (int mi = 0; mi < 2; ++mi)
#pragma unroll
    for (int ni = 0; ni < 9; ++ni) *(f32x4*)(dst + (mi * 9 + ni) * 256) = acc[mi][ni];
}

__global__ __launch_bounds__(256) void k_final(const float* __restrict__ part, const u16* __restrict__ x,
                                               const u16* __restrict__ b2, u16* __restrict__ out) {
  const int gid = blockIdx.x * 256 + threadIdx.x;
  const int lane = gid & 63;
  int r = gid >> 6;
  const int fi = r % 18; r /= 18;
  const int wave = r & 3; const int tile = r >> 2;
  const int pm = tile >> 2, pn = tile & 3, mi = fi / 9, ni = fi % 9;
  const int m = pm * 128 + wave * 32 + mi * 16 + (lane & 15);
  const int n = pn * 144 + ni * 16 + 4 * (lane >> 4);
  const size_t PS = (size_t)S_ * D_;
  const f32x4 p0 = *(const f32x4*)(part + (size_t)gid * 4);
  const f32x4 p1 = *(const f32x4*)(part + PS + (size_t)gid * 4);
  const f32x4 p2 = *(const f32x4*)(part + 2 * PS + (size_t)gid * 4);
  const f32x4 p3 = *(const f32x4*)(part + 3 * PS + (size_t)gid * 4);
  const u32x2 xb = *(const u32x2*)(x + (size_t)m * D_ + n);
  const u32x2 bb = *(const u32x2*)(b2 + n);
  const float xv[4] = {lo_f(xb[0]), hi_f(xb[0]), lo_f(xb[1]), hi_f(xb[1])};
  const float bv[4] = {lo_f(bb[0]), hi_f(bb[0]), lo_f(bb[1]), hi_f(bb[1])};
  float o[4];
#pragma unroll
  for (int i = 0; i < 4; ++i) {
    const float s = rbf(rbf(p0[i]) - rbf(p1[i]));
    const float z = rbf(rbf(p2[i] + p3[i]) + bv[i]);
    const float g = rbf(z * __builtin_amdgcn_rcpf(1.0f + __builtin_amdgcn_exp2f(-1.4426950408889634f * z)));
    o[i] = xv[i] + rbf(s * g);
  }
  u32x2 w = {pk_bf16(o[0], o[1]), pk_bf16(o[2], o[3])};
  *(u32x2*)(out + (size_t)m * D_ + n) = w;
}

extern "C" void kernel_launch(void* const* d_in, const int* in_sizes, int n_in, void* d_out, int out_size, void* d_ws,
                              size_t ws_size, hipStream_t stream) {
  const u16* x = (const u16*)d_in[0];
  const u16* ws_re = (const u16*)d_in[1];
  const u16* ws_im = (const u16*)d_in[2];
  const u16* wd_re = (const u16*)d_in[3];
  const u16* wd_im = (const u16*)d_in[4];
  const u16* w1 = (const u16*)d_in[6];
  const u16* b1 = (const u16*)d_in[7];
  const u16* w2 = (const u16*)d_in[8];
  const u16* b2 = (const u16*)d_in[9];
  u16* out = (u16*)d_out;
  char* ws = (char*)d_ws;
  const size_t SD = (size_t)S_ * D_;
  u16* xn = (u16*)ws;
  u16* y = xn + SD;
  u16* abT = y + SD;
  u16* h = abT + 2 * SD;
  float* part = (float*)(h + (size_t)S_ * F_);
  k_ln<<<S_ / 4, 256, 0, stream>>>(x, xn, y);
  k_gemm1<<<240, 512, 0, stream>>>(xn, y, wd_re, wd_im, w1, b1, abT, h);
  k_gemm2<<<256, 256, 0, stream>>>(ws_re, ws_im, abT, h, w2, part);
  k_final<<<(int)(SD / 4 / 256), 256, 0, stream>>>(part, x, b2, out);
}
```

```cpp
#include <hip/hip_runtime.h>
#include <hip/hip_bf16.h>
#include <stdint.h>
#include <type_traits>

typedef unsigned short u16;
typedef short bf16x8 __attribute__((ext_vector_type(8)));
typedef short s16x4 __attribute__((ext_vector_type(4)));
typedef float f32x4 __attribute__((ext_vector_type(4)));
typedef unsigned u32x4 __attribute__((ext_vector_type(4)));
typedef unsigned u32x2 __attribute__((ext_vector_type(2)));
#define LAS __attribute__((address_space(3)))
#define GAS __attribute__((address_space(1)))
#define DI __device__ __forceinline__

constexpr int S_ = 2048, D_ = 576, F_ = 4608;

#define STAMP(kid, idx) do { } while (0)
#define STAMPB(kid, blk, idx) do { } while (0)

DI float bf2f(unsigned b) { return __uint_as_float(b << 16); }
typedef __bf16 bf16x2v __attribute__((ext_vector_type(2)));
typedef float f32x2 __attribute__((ext_vector_type(2)));
DI unsigned pk_bf16(float lo, float hi) {
  bf16x2v r = __builtin_convertvector((f32x2){lo, hi}, bf16x2v);
  return __builtin_bit_cast(unsigned, r);
}
DI float rbf(float x) { return __uint_as_float(pk_bf16(x, x) << 16); }
DI float lo_f(unsigned p) { return __uint_as_float(p << 16); }
DI float hi_f(unsigned p) { return __uint_as_float(p & 0xffff0000u); }

template <int N> DI void wait_vm() { asm volatile("s_waitcnt vmcnt(%0)" ::"n"(N) : "memory"); }
DI void raw_barrier() {
  asm volatile("s_waitcnt lgkmcnt(0)" ::: "memory");
  __builtin_amdgcn_s_barrier();
  asm volatile("" ::: "memory");
}

DI float wave_sum(float v) {
#pragma unroll
  for (int o = 32; o > 0; o >>= 1) v += __shfl_xor(v, o);
  return v;
}

DI void ln_row(float (&v)[16], bool has2) {
  float s = 0.f;
#pragma unroll
  for (int i = 0; i < 8; ++i) s += v[i];
  if (has2) {
#pragma unroll
    for (int i = 8; i < 16; ++i) s += v[i];
  }
  s = wave_sum(s);
  const float mean = s * (1.0f / D_);
  float q = 0.f;
#pragma unroll
  for (int i = 0; i < 8; ++i) { float d = v[i] - mean; q += d * d; }
  if (has2) {
#pragma unroll
    for (int i = 8; i < 16; ++i) { float d = v[i] - mean; q += d * d; }
  }
  q = wave_sum(q);
  const float var = rbf(q * (1.0f / D_));
  const float mb = rbf(mean);
  const float den = rbf(sqrtf(rbf(var + 1e-5f)));
#pragma unroll
  for (int i = 0; i < 16; ++i) v[i] = rbf(rbf(v[i] - mb) / den);
}

__global__ __launch_bounds__(256) void k_ln(const u16* __restrict__ x, u16* __restrict__ xn, u16* __restrict__ y,
                                            const u16* __restrict__ w1, const u16* __restrict__ wd_re, const u16* __restrict__ wd_im) {
  const int lane = threadIdx.x & 63;
  const int row = blockIdx.x * 4 + (threadIdx.x >> 6);
  if ((blockIdx.x & 1) == 0) STAMPB(0, blockIdx.x >> 1, 0);
  const bool has2 = lane < 8;
  const u16* xr = x + (size_t)row * D_;
  u32x4 p0 = *(const u32x4*)(xr + lane * 8);
  u32x4 p1 = {0u, 0u, 0u, 0u};
  if (has2) p1 = *(const u32x4*)(xr + 512 + lane * 8);
  {
    __shared__ __attribute__((aligned(16))) char scr[256];
    const unsigned scra = (unsigned)(size_t)(LAS char*)scr;
    const int li = blockIdx.x * 256 + threadIdx.x;
    if (li < 41472 + 2 * 5184) {
      const char* p = li < 41472 ? (const char*)w1 + (size_t)li * 128 : (li < 41472 + 5184 ? (const char*)wd_re + (size_t)(li - 41472) * 128 : (const char*)wd_im + (size_t)(li - 41472 - 5184) * 128);
      asm volatile("s_mov_b32 m0, %0\n\ts_nop 0\n\tglobal_load_lds_dword %1, off" ::"s"(scra), "v"(p) : "memory");
    }
  }
  float v[16];
#pragma unroll
  for (int i = 0; i < 4; ++i) { v[2 * i] = lo_f(p0[i]); v[2 * i + 1] = hi_f(p0[i]); v[8 + 2 * i] = lo_f(p1[i]); v[9 + 2 * i] = hi_f(p1[i]); }
  ln_row(v, has2);
#pragma unroll
  for (int i = 0; i < 4; ++i) { p0[i] = pk_bf16(v[2 * i], v[2 * i + 1]); p1[i] = pk_bf16(v[8 + 2 * i], v[9 + 2 * i]); }
  *(u32x4*)(xn + (size_t)row * D_ + lane * 8) = p0;
  if (has2) *(u32x4*)(xn + (size_t)row * D_ + 512 + lane * 8) = p1;
  ln_row(v, has2);
#pragma unroll
  for (int i = 0; i < 4; ++i) { p0[i] = pk_bf16(v[2 * i], v[2 * i + 1]); p1[i] = pk_bf16(v[8 + 2 * i], v[9 + 2 * i]); }
  *(u32x4*)(y + (size_t)row * D_ + lane * 8) = p0;
  if (has2) *(u32x4*)(y + (size_t)row * D_ + 512 + lane * 8) = p1;
  if ((blockIdx.x & 1) == 0) STAMPB(0, blockIdx.x >> 1, 2);
}

template <int I, int N, class F> DI void static_for(F&& f) {
  if constexpr (I < N) { f(std::integral_constant<int, I>{}); static_for<I + 1, N>(f); }
}
template <int OFF> DI void ds_rd128(bf16x8& d, unsigned addr) {
  asm volatile("ds_read_b128 %0, %1 offset:%2" : "=v"(d) : "v"(addr), "i"(OFF));
}
template <int OFF> DI void ds_rdtr(s16x4& d, unsigned addr) {
  asm volatile("ds_read_b64_tr_b16 %0, %1 offset:%2" : "=v"(d) : "v"(addr), "i"(OFF));
}
DI void glds16(unsigned lds, unsigned voff, const char* sbase) {
  asm volatile("s_mov_b32 m0, %0\n\ts_nop 0\n\tglobal_load_lds_dwordx4 %1, %2" ::"s"(lds), "v"(voff), "s"(sbase) : "memory");
}
DI void glds16_half(unsigned lds, unsigned voff, const char* sbase) {
  asm volatile("s_mov_b32 m0, %0\n\ts_mov_b32 exec_hi, 0\n\tglobal_load_lds_dwordx4 %1, %2\n\ts_mov_b32 exec_hi, -1" ::"s"(lds), "v"(voff), "s"(sbase) : "memory");
}
template <class T> DI void tie(T& v) { asm volatile("" : "+v"(v)); }
DI void sched_fence() { __builtin_amdgcn_sched_barrier(0); }

DI float gelu_f(float x) {
  const float c0 = -2.0f * 0.7978845608028654f * 1.4426950408889634f;
  const float c1 = c0 * 0.044715f;
  float z = x * __builtin_fmaf(x * x, c1, c0);
  float e = __builtin_amdgcn_exp2f(z);
  return x * __builtin_amdgcn_rcpf(1.0f + e);
}

#define AUX0 1
constexpr int K2_NCH = 20, K2_NK = 9, K2_TOT = 7 * K2_NK;
DI int nn_u(int k) { return ((k >> 1) & 1) | (((k >> 3) & 1) << 1); }
DI int w2_pos(int k) { return (k & ~12) | ((k & 4) << 1) | ((k & 8) >> 1); }

DI u32x4 retile_ld(const u16* w2, int c) { return *(const u32x4*)(w2 + (size_t)c * 8); }
DI void retile_st(u16* w2t, int c, u32x4 v) {
  const int k = c / 72, ch = c % 72, pn = ch / 18, cc = ch % 18;
  *(u32x4*)(w2t + ((size_t)pn * F_ + w2_pos(k)) * 144 + cc * 8) = v;
}
DI void retile(const u16* w2, u16* w2t, int c) { retile_st(w2t, c, retile_ld(w2, c)); }
DI unsigned vadd_s(unsigned v, unsigned sc) { unsigned r; asm volatile("v_add_u32 %0, %1, %2" : "=v"(r) : "s"(sc), "v"(v)); return r; }
DI unsigned vxad_s(unsigned v, unsigned sc) { unsigned r; asm volatile("v_xad_u32 %0, %1, 64, %2" : "=v"(r) : "v"(v), "s"(sc)); return r; }
DI void touch1(unsigned& sink, const char* p) { asm volatile("global_load_dword %0, %1, off" : "+v"(sink) : "v"(p)); }
struct K2Aux { const char* ws_re; const char* ws_im; const char* w2; char* w2t; int blk; };

template <bool BNN, bool SWAP, int ROLE>
DI void k2_main(unsigned lds0, int wave, const char* gA, const char* gB, int ldb_b, const K2Aux& aux, f32x4 (&acc)[4][6]) {
  const int lane = threadIdx.x & 63, fr = lane & 15, fq = lane >> 4;
  const int wm = wave >> 1, wn = wave & 1;
  unsigned vA[2], vB[2];
  if (ROLE == 0) {
#pragma unroll
    for (int i = 0; i < 2; ++i) {
      const int bb = (wave * 2 + i) * 1024 + lane * 16, r = bb >> 7, c = (bb >> 4) & 7;
      vA[i] = (unsigned)(r * (D_ * 2) + ((c ^ ((r >> 1) & 7)) * 16));
      if (!BNN) vB[i] = (unsigned)(r * ldb_b + ((c ^ ((r >> 1) & 7)) * 16));
      else vB[i] = (unsigned)(r * ldb_b + ((c ^ (2 * nn_u(r))) * 16));
    }
  }
  const unsigned ldsw = lds0 + (unsigned)(wave * 2048);
  auto issue = [&](auto gc, auto ic) {
    constexpr int G = decltype(gc)::value, I = decltype(ic)::value, ST = G / 7, C = G % 7, SLOT = G % K2_NCH;
    if constexpr (ROLE == 0 && G < K2_TOT) {
      if constexpr (C < 4) glds16(ldsw + (unsigned)(SLOT * 8192 + I * 1024), vA[I], gA + (C * 64 * D_ * 2 + ST * 128));
      else if constexpr (!BNN) glds16(ldsw + (unsigned)(SLOT * 8192 + I * 1024), vB[I], gB + ((long)(C - 4) * 64 * (D_ * 2) + ST * 128));
      else glds16(ldsw + (unsigned)(SLOT * 8192 + I * 1024), vB[I], gB + ((long)ST * 64 * (F_ * 2) + (C - 4) * 128));
    }
  };
  unsigned sink = 0;
  const int t4 = threadIdx.x - 256;
  auto aux_step = [&](auto sc) {
    constexpr int STEP = decltype(sc)::value;
    if constexpr (ROLE == 1) {
      const int li = STEP * (240 * 256) + aux.blk * 256 + t4;
      if (li < 41472 + 131072) {
        const char* p = li < 41472 ? aux.w2 + (size_t)li * 128 : (li < 41472 + 65536 ? aux.ws_re + (size_t)(li - 41472) * 128 : aux.ws_im + (size_t)(li - 41472 - 65536) * 128);
        touch1(sink, p);
      }
    }
  };
  unsigned aoff[2], boff[6];
#pragma unroll
  for (int kk = 0; kk < 2; ++kk) aoff[kk] = lds0 + (unsigned)(fr * 128 + (((kk * 4 + fq) ^ ((fr >> 1) & 7)) * 16));
#pragma unroll
  for (int ni = 0; ni < 6; ++ni) {
    const int f4 = (wn * 6 + ni) & 3;
    if (!BNN) boff[ni] = lds0 + (unsigned)((f4 * 16 + fr) * 128 + ((fq ^ ((fr >> 1) & 7)) * 16));
    else {
      const int g = lane >> 4, q = (lane & 15) >> 2, p = lane & 3, k = 8 * g + q;
      boff[ni] = lds0 + (unsigned)(k * 128 + ((2 * (f4 ^ nn_u(k)) + (p >> 1)) * 16) + (p & 1) * 8);
    }
  }
  struct Fr { bf16x8 a[4]; bf16x8 b[6]; s16x4 bl[6], bh[6]; };
  Fr f0, f1;
  constexpr int NRD = 4 + (BNN ? 12 : 6);
  unsigned ra, rb[6];
  auto set_addr = [&](auto sc, auto kc) {
    constexpr int ST = decltype(sc)::value, KK = decltype(kc)::value;
    int sa = (7 * ST) % K2_NCH + wm; if (sa >= K2_NCH) sa -= K2_NCH;
    ra = vadd_s(aoff[KK], (unsigned)(sa * 8192));
    constexpr unsigned S0 = ((7 * ST + 4) % K2_NCH) * 8192, S1 = ((7 * ST + 5) % K2_NCH) * 8192, S2 = ((7 * ST + 6) % K2_NCH) * 8192;
    const unsigned lo = wn ? S1 : S0, hi = wn ? S2 : S1, mid = wn ? S2 : S0;
#pragma unroll
    for (int ni = 0; ni < 6; ++ni) {
      const unsigned sb = ni < 2 ? lo : (ni < 4 ? mid : hi);
      if (!BNN && KK == 1) rb[ni] = vxad_s(boff[ni], sb); else rb[ni] = vadd_s(boff[ni], sb);
    }
  };
  auto rd = [&](auto kc, auto jc, Fr& f) {
    constexpr int KK = decltype(kc)::value, J = decltype(jc)::value;
    if constexpr (J < 4) ds_rd128<J * 2048>(f.a[J], ra);
    else if constexpr (!BNN) ds_rd128<0>(f.b[J - 4], rb[J - 4]);
    else {
      constexpr int ni = (J - 4) >> 1, t = (J - 4) & 1;
      if constexpr (t == 0) ds_rdtr<KK * 4096>(f.bl[ni], rb[ni]); else ds_rdtr<KK * 4096 + 512>(f.bh[ni], rb[ni]);
    }
  };
  auto waitf = [&](Fr& f) {
    asm volatile("s_waitcnt lgkmcnt(0)" ::: "memory");
#pragma unroll
    for (int i = 0; i < 4; ++i) tie(f.a[i]);
#pragma unroll
    for (int i = 0; i < 6; ++i) { if (!BNN) tie(f.b[i]); else { tie(f.bl[i]); tie(f.bh[i]); } }
  };
  auto mm = [&](auto ic, const Fr& f) {
    constexpr int I = decltype(ic)::value, mi = I / 6, ni = I % 6;
    bf16x8 bb;
    if constexpr (!BNN) bb = f.b[ni]; else bb = __builtin_shufflevector(f.bl[ni], f.bh[ni], 0, 1, 2, 3, 4, 5, 6, 7);
    if (SWAP) acc[mi][ni] = __builtin_amdgcn_mfma_f32_16x16x32_bf16(bb, f.a[mi], acc[mi][ni], 0, 0, 0);
    else acc[mi][ni] = __builtin_amdgcn_mfma_f32_16x16x32_bf16(f.a[mi], bb, acc[mi][ni], 0, 0, 0);
  };
  auto substep = [&](Fr& fc, Fr& fn, auto rsc, auto rkc, auto p0c, auto p1c) {
    constexpr int RS = decltype(rsc)::value, RK = decltype(rkc)::value, P0 = decltype(p0c)::value, P1 = decltype(p1c)::value;
    if constexpr (RS >= 0) set_addr(std::integral_constant<int, (RS >= 0 ? RS : 0)>{}, rkc);
    sched_fence();
    static_for<0, 24>([&](auto ic) {
      constexpr int I = decltype(ic)::value;
      mm(ic, fc);
      if constexpr (RS >= 0 && I < NRD) rd(rkc, ic, fn);
      constexpr int NP = P1 - P0;
      if constexpr (I >= 24 - 2 * NP && ((24 - I) % 2) == 0) {
        constexpr int P = P1 - (24 - I) / 2;
        issue(std::integral_constant<int, P / 2>{}, std::integral_constant<int, P % 2>{});
      }
      sched_fence();
    });
  };
  using IC = std::integral_constant<int, 0>;
  (void)sizeof(IC);
  static_for<0, K2_NCH>([&](auto gc) { issue(gc, std::integral_constant<int, 0>{}); issue(gc, std::integral_constant<int, 1>{}); });
  if (ROLE == 0) wait_vm<(K2_NCH - 7) * 2>();
  raw_barrier();
  STAMP(1, 3);
  set_addr(std::integral_constant<int, 0>{}, std::integral_constant<int, 0>{});
  static_for<0, NRD>([&](auto jc) { rd(std::integral_constant<int, 0>{}, jc, f0); });
  static_for<0, K2_NK>([&](auto sc) {
    constexpr int K = decltype(sc)::value;
    constexpr int PB = 2 * (7 * (K - 1) + K2_NCH);
    waitf(f0);
    if constexpr (K >= 1) substep(f0, f1, sc, std::integral_constant<int, 1>{}, std::integral_constant<int, PB + 7>{}, std::integral_constant<int, PB + 14>{});
    else substep(f0, f1, sc, std::integral_constant<int, 1>{}, std::integral_constant<int, 0>{}, std::integral_constant<int, 0>{});
    waitf(f1);
    if constexpr (K < K2_NK - 1) {
      constexpr int ISS = (7 * K + K2_NCH - 1 < K2_TOT - 1) ? 7 * K + K2_NCH - 1 : K2_TOT - 1;
      if (ROLE == 0) wait_vm<(ISS - (7 * K + 13)) * 2>();
      __builtin_amdgcn_s_barrier();
      asm volatile("" ::: "memory");
      if constexpr (K >= AUX0 && K < AUX0 + 3) aux_step(std::integral_constant<int, K - AUX0>{});
      constexpr int PC = 2 * (7 * K + K2_NCH);
      substep(f1, f0, std::integral_constant<int, K + 1>{}, std::integral_constant<int, 0>{}, std::integral_constant<int, PC>{}, std::integral_constant<int, PC + 7>{});
    } else {
      substep(f1, f0, std::integral_constant<int, -1>{}, std::integral_constant<int, 0>{}, std::integral_constant<int, 0>{}, std::integral_constant<int, 0>{});
    }
  });
  if (ROLE == 1) { asm volatile("s_waitcnt vmcnt(0)" : "+v"(sink)); }
}

__global__ __launch_bounds__(512, 2) void k_gemm1(const u16* __restrict__ xn, const u16* __restrict__ y,
                                                  const u16* __restrict__ wd_re, const u16* __restrict__ wd_im,
                                                  const u16* __restrict__ w1, const u16* __restrict__ b1,
                                                  u16* __restrict__ abT, u16* __restrict__ h,
                                                  const u16* __restrict__ w2, u16* __restrict__ w2t, unsigned* __restrict__ flags,
                                                  const u16* __restrict__ ws_re, const u16* __restrict__ ws_im) {
  __shared__ __attribute__((aligned(1024))) char smem_[K2_NCH * 8192];
  LAS char* smem = (LAS char*)smem_;
  STAMP(1, 0);
  const int b = blockIdx.x;
  if (b >= 240) {
    if (b == 240 && threadIdx.x < 256) flags[threadIdx.x * 64] = 0u;
#pragma unroll
    for (int i = 0; i < 11; ++i) {
      const int o = i * 512 + threadIdx.x;
      if (o < 5376) retile(w2, w2t, 240 * 1024 + (b - 240) * 5376 + o);
    }
    return;
  }
  const int t = (b & 7) * 30 + (b >> 3);
  const int pm = t & 7, pn = t >> 3;
  const int lane = threadIdx.x & 63, fr = lane & 15, fq = lane >> 4;
  const int wave = __builtin_amdgcn_readfirstlane(threadIdx.x >> 6);
  const int wm = wave >> 1, wn = wave & 1;
  const unsigned lds0 = (unsigned)(size_t)smem;
  K2Aux aux{(const char*)ws_re, (const char*)ws_im, (const char*)w2, (char*)w2t, b};
  f32x4 acc[4][6];
#pragma unroll
  for (int i = 0; i < 4; ++i)
#pragma unroll
    for (int j = 0; j < 6; ++j) acc[i][j] = (f32x4){0.f, 0.f, 0.f, 0.f};
  if (pn < 6) {
    const u16* wd = pn < 3 ? wd_re : wd_im;
    const int n0 = (pn < 3 ? pn : pn - 3) * 192;
    const char* gA = (const char*)(xn + (size_t)pm * 256 * D_);
    const char* gB = (const char*)(wd + (size_t)n0 * D_);
    if (wave < 4) k2_main<false, false, 0>(lds0, wave, gA, gB, D_ * 2, aux, acc);
    else k2_main<false, false, 1>(lds0, wave, gA, gB, D_ * 2, aux, acc);
    STAMP(1, 1);
    const u32x4 c0 = retile_ld(w2, b * 1024 + threadIdx.x), c1 = retile_ld(w2, b * 1024 + 512 + threadIdx.x);
    sched_fence();
    u16* dst = abT + (size_t)(pn < 3 ? 0 : 1) * D_ * S_;
    const auto ars = __builtin_amdgcn_make_buffer_rsrc(dst, 0, D_ * S_ * 2, 0x00020000);
#pragma unroll
    for (int ni = 0; ni < 6; ++ni) {
      const int e = n0 + wn * 96 + ni * 16 + fr;
#pragma unroll
      for (int mp = 0; mp < 2; ++mp) {
        unsigned x0 = pk_bf16(acc[2 * mp][ni][0], acc[2 * mp][ni][1]), x1 = pk_bf16(acc[2 * mp][ni][2], acc[2 * mp][ni][3]);
        unsigned y0 = pk_bf16(acc[2 * mp + 1][ni][0], acc[2 * mp + 1][ni][1]), y1 = pk_bf16(acc[2 * mp + 1][ni][2], acc[2 * mp + 1][ni][3]);
        auto r0 = __builtin_amdgcn_permlane16_swap(x0, y0, false, false);
        auto r1 = __builtin_amdgcn_permlane16_swap(x1, y1, false, false);
        u32x4 w = {r0[0], r1[0], r0[1], r1[1]};
        const int s = pm * 256 + wm * 64 + (2 * mp + (fq & 1)) * 16 + (fq >> 1) * 8;
        __builtin_amdgcn_raw_buffer_store_b128(w, ars, (e * S_ + s) * 2, 0, 16);
      }
    }
    sched_fence();
    retile_st(w2t, b * 1024 + threadIdx.x, c0); retile_st(w2t, b * 1024 + 512 + threadIdx.x, c1);
  } else {
    const int n0 = (pn - 6) * 192;
    const char* gA = (const char*)(y + (size_t)pm * 256 * D_);
    const char* gB = (const char*)(w1 + n0);
    if (wave < 4) k2_main<true, true, 0>(lds0, wave, gA, gB, F_ * 2, aux, acc);
    else k2_main<true, true, 1>(lds0, wave, gA, gB, F_ * 2, aux, acc);
    STAMP(1, 1);
    const u32x4 c0 = retile_ld(w2, b * 1024 + threadIdx.x), c1 = retile_ld(w2, b * 1024 + 512 + threadIdx.x);
    sched_fence();
    const auto hrs = __builtin_amdgcn_make_buffer_rsrc(h, 0, F_ * S_ * 2, 0x00020000);
    float bias[6][4];
#pragma unroll
    for (int ni = 0; ni < 6; ++ni) {
      u32x2 p = *(const u32x2*)(b1 + n0 + wn * 96 + ni * 16 + 4 * fq);
      bias[ni][0] = lo_f(p[0]); bias[ni][1] = hi_f(p[0]); bias[ni][2] = lo_f(p[1]); bias[ni][3] = hi_f(p[1]);
    }
#pragma unroll
    for (int mi = 0; mi < 4; ++mi) {
      const int m = pm * 256 + wm * 64 + mi * 16 + fr;
#pragma unroll
      for (int np = 0; np < 3; ++np) {
        unsigned pk[2][2];
#pragma unroll
        for (int o = 0; o < 2; ++o) {
          const int ni = 2 * np + o;
          float v[4];
#pragma unroll
          for (int j = 0; j < 4; ++j) v[j] = gelu_f(rbf(rbf(acc[mi][ni][j]) + bias[ni][j]));
          pk[o][0] = pk_bf16(v[0], v[1]); pk[o][1] = pk_bf16(v[2], v[3]);
        }
        auto r0 = __builtin_amdgcn_permlane16_swap(pk[0][0], pk[1][0], false, false);
        auto r1 = __builtin_amdgcn_permlane16_swap(pk[0][1], pk[1][1], false, false);
        u32x4 w = {r0[0], r1[0], r0[1], r1[1]};
        const int f = n0 + wn * 96 + (2 * np + (fq & 1)) * 16 + (fq >> 1) * 8;
        __builtin_amdgcn_raw_buffer_store_b128(w, hrs, (m * F_ + f) * 2, 0, 16);
      }
    }
    sched_fence();
    retile_st(w2t, b * 1024 + threadIdx.x, c0); retile_st(w2t, b * 1024 + 512 + threadIdx.x, c1);
  }
  STAMP(1, 2);
}

constexpr int K3_NS = 4, K3_A = 16384, K3_B = 18432, K3_STAGE = K3_A + K3_B, K3_CNT = 9;
constexpr int FLAG_STRIDE = 64;

template <bool BNN, int NK>
DI void k3_main(unsigned lds0, int wave, const char* gA, int lda_b, const char* gB, int ldb_b, long bstep, int rot, f32x4 (&acc)[4][9]) {
  const int lane = threadIdx.x & 63;
  constexpr int NS = K3_NS, STAGE = K3_STAGE, CNT = K3_CNT;
  if (wave >= 4) {
    const int lw = wave - 4;
    unsigned voff[CNT];
#pragma unroll
    for (int i = 0; i < 4; ++i) {
      int b = (lw * 4 + i) * 1024 + lane * 16;
      int r = b >> 7, c = (b >> 4) & 7;
      voff[i] = (unsigned)(r * lda_b + ((c ^ ((r >> 1) & 7)) * 16));
    }
#pragma unroll
    for (int i = 0; i < 5; ++i) {
      int b = lw * 4608 + i * 1024 + ((i < 4) ? lane : (lane & 31)) * 16;
      if (!BNN) {
        int r = b >> 7, c = (b >> 4) & 7;
        voff[4 + i] = (unsigned)(r * ldb_b + ((c ^ ((r >> 1) & 7)) * 16));
      } else voff[4 + i] = (unsigned)b;
    }
    int kidx = rot;
    const char* pA = gA + (long)kidx * 128;
    const char* pB = gB + (long)kidx * bstep;
    auto stage = [&](unsigned slot) {
#pragma unroll
      for (int i = 0; i < 4; ++i) glds16(lds0 + slot + (unsigned)((lw * 4 + i) * 1024), voff[i], pA);
#pragma unroll
      for (int i = 0; i < 4; ++i) glds16(lds0 + slot + (unsigned)(K3_A + lw * 4608 + i * 1024), voff[4 + i], pB);
      glds16_half(lds0 + slot + (unsigned)(K3_A + lw * 4608 + 4096), voff[8], pB);
      ++kidx; pA += 128; pB += bstep;
      if (kidx == NK) { kidx = 0; pA = gA; pB = gB; }
    };
#pragma unroll
    for (int s = 0; s < NS; ++s) stage((unsigned)(s * STAGE));
    wait_vm<(NS - 1) * CNT>();
    raw_barrier();
    unsigned cur = 0;
    for (int k = 0; k < NK - NS; ++k) {
      wait_vm<(NS - 2) * CNT>();
      raw_barrier();
      stage(cur);
      cur += STAGE; if (cur == NS * STAGE) cur = 0;
    }
    static_for<0, NS - 1>([&](auto tc) {
      constexpr int REM = NS - 1 - decltype(tc)::value;
      wait_vm<(REM - 1) * CNT>();
      raw_barrier();
    });
    return;
  }
  const int wm = wave & 1, kh = wave >> 1, fr = lane & 15, fq = lane >> 4;
  const unsigned aoff = lds0 + (unsigned)((wm * 64 + fr) * 128 + (((kh * 4 + fq) ^ ((fr >> 1) & 7)) * 16));
  unsigned boff;
  if (!BNN) boff = lds0 + (unsigned)(K3_A + fr * 128 + (((kh * 4 + fq) ^ ((fr >> 1) & 7)) * 16));
  else {
    const int g = lane >> 4, q = (lane & 15) >> 2, p = lane & 3;
    boff = lds0 + (unsigned)(K3_A + (kh * 32 + (g >> 1) * 16 + (g & 1) * 4 + q) * 288 + (p >> 1) * 16 + (p & 1) * 8);
  }
  bf16x8 a0[4], a1[4], b[9];
  s16x4 bl[9], bh[9];
  auto rdA = [&](auto ic, bf16x8 (&a)[4], unsigned ad) { constexpr int I = decltype(ic)::value; ds_rd128<I * 2048>(a[I], ad); };
  auto rdB = [&](auto ic, unsigned ad) {
    constexpr int I = decltype(ic)::value;
    if constexpr (!BNN) ds_rd128<I * 2048>(b[I], ad);
    else { ds_rdtr<I * 32>(bl[I], ad); ds_rdtr<I * 32 + 8 * 288>(bh[I], ad); }
  };
  auto tieB = [&](auto ic) {
    constexpr int I = decltype(ic)::value;
    if constexpr (!BNN) tie(b[I]); else { tie(bl[I]); tie(bh[I]); }
  };
  auto mm = [&](auto ic, const bf16x8 (&a)[4]) {
    constexpr int I = decltype(ic)::value, ni = I / 4, mi = I % 4;
    bf16x8 bb;
    if constexpr (!BNN) bb = b[ni]; else bb = __builtin_shufflevector(bl[ni], bh[ni], 0, 1, 2, 3, 4, 5, 6, 7);
    acc[mi][ni] = __builtin_amdgcn_mfma_f32_16x16x32_bf16(bb, a[mi], acc[mi][ni], 0, 0, 0);
  };
  constexpr int NT = BNN ? 4 : 2;
  auto body = [&](bf16x8 (&aC)[4], bf16x8 (&aN)[4], unsigned nslot, auto lastc) {
    constexpr bool LAST = decltype(lastc)::value;
    asm volatile("s_waitcnt lgkmcnt(%0)" ::"n"(NT) : "memory");
    static_for<0, 4>([&](auto ic) { tie(aC[decltype(ic)::value]); });
    static_for<0, 7>([&](auto ic) { tieB(ic); });
    sched_fence();
    static_for<0, 8>([&](auto ic) { mm(ic, aC); sched_fence(); });
    asm volatile("s_waitcnt lgkmcnt(0)" ::: "memory");
    tieB(std::integral_constant<int, 7>{}); tieB(std::integral_constant<int, 8>{});
    if constexpr (!LAST) { __builtin_amdgcn_s_barrier(); asm volatile("" ::: "memory"); }
    const unsigned ad = aoff + nslot, bd = boff + nslot;
    sched_fence();
    static_for<8, 36>([&](auto ic) {
      constexpr int I = decltype(ic)::value;
      mm(ic, aC);
      if constexpr (!LAST) {
        if constexpr (I >= 8 && I < 12) rdA(std::integral_constant<int, I - 8>{}, aN, ad);
        if constexpr (I == 12) rdB(std::integral_constant<int, 0>{}, bd);
        if constexpr (I == 13) rdB(std::integral_constant<int, 1>{}, bd);
        if constexpr (I == 14) rdB(std::integral_constant<int, 2>{}, bd);
        if constexpr (I >= 15 && (I % 4) == 3) rdB(std::integral_constant<int, I / 4>{}, bd);
      }
      sched_fence();
    });
  };
  raw_barrier();
  static_for<0, 4>([&](auto ic) { rdA(ic, a0, aoff); });
  static_for<0, 9>([&](auto ic) { rdB(ic, boff); });
  unsigned nslot = STAGE;
  for (int k = 0; k < NK - 2; k += 2) {
    body(a0, a1, nslot, std::false_type{});
    nslot += STAGE; if (nslot == NS * STAGE) nslot = 0;
    body(a1, a0, nslot, std::false_type{});
    nslot += STAGE; if (nslot == NS * STAGE) nslot = 0;
  }
  body(a0, a1, nslot, std::false_type{});
  body(a1, a0, 0u, std::true_type{});
}

typedef GAS unsigned gu32;
#define RLX_AGENT __ATOMIC_RELAXED, __HIP_MEMORY_SCOPE_AGENT

__global__ __launch_bounds__(512, 2) void k_gemm2(const u16* __restrict__ ws_re, const u16* __restrict__ ws_im,
                                                  const u16* __restrict__ abT, const u16* __restrict__ h,
                                                  const u16* __restrict__ w2t, float* part, unsigned* flags,
                                                  const u16* __restrict__ x, const u16* __restrict__ b2, u16* __restrict__ out) {
  __shared__ __attribute__((aligned(1024))) char smem_[K3_NS * K3_STAGE];
  LAS char* smem = (LAS char*)smem_;
  STAMP(2, 0);
  const int blk = blockIdx.x;
  const int xcd = blk & 7, j = blk >> 3;
  const int ks = xcd >> 1, pm = (xcd & 1) * 8 + (j >> 2), pn = j & 3;
  const int tile = pm * 4 + pn;
  const int tid = threadIdx.x, lane = tid & 63;
  const int wave = __builtin_amdgcn_readfirstlane(tid >> 6);
  const unsigned lds0 = (unsigned)(size_t)smem;
  f32x4 acc[4][9];
#pragma unroll
  for (int i = 0; i < 4; ++i)
#pragma unroll
    for (int jj = 0; jj < 9; ++jj) acc[i][jj] = (f32x4){0.f, 0.f, 0.f, 0.f};
  if (ks < 2) {
    const char* gA = (const char*)((ks == 0 ? ws_re : ws_im) + (size_t)pm * 128 * S_);
    const char* gB = (const char*)(abT + (size_t)ks * D_ * S_ + (size_t)pn * 144 * S_);
    k3_main<false, S_ / 64>(lds0, wave, gA, S_ * 2, gB, S_ * 2, 128, (xcd & 1) * 16, acc);
  } else {
    const int k0 = (ks - 2) * (F_ / 2);
    const char* gA = (const char*)(h + (size_t)pm * 128 * F_ + k0);
    const char* gB = (const char*)(w2t + ((size_t)pn * F_ + k0) * 144);
    k3_main<true, F_ / 2 / 64>(lds0, wave, gA, F_ * 2, gB, 0, K3_B, (xcd & 1) * 18, acc);
  }
  STAMP(2, 1);
  __syncthreads();
  LAS f32x4* X = (LAS f32x4*)smem;
  LAS f32x4* Y = X + 4 * 18 * 64;
  const int wm = wave & 1, kh = (wave >> 1) & 1;
  if (wave < 4) {
    if (kh) {
#pragma unroll
      for (int i = 0; i < 2; ++i)
#pragma unroll
        for (int ni = 0; ni < 9; ++ni) X[(wave * 18 + i * 9 + ni) * 64 + lane] = acc[i][ni];
    } else {
#pragma unroll
      for (int i = 0; i < 2; ++i)
#pragma unroll
        for (int ni = 0; ni < 9; ++ni) X[(wave * 18 + i * 9 + ni) * 64 + lane] = acc[2 + i][ni];
    }
  }
  __syncthreads();
  const auto prs = __builtin_amdgcn_make_buffer_rsrc(part, 0, 64 * 16 * 18 * 1024, 0x00020000);
  if (wave < 4) {
    const int q = 2 * wm + kh;
    const int base = ((tile * 4 + q) * 4 + ks) * 18 * 1024;
    auto fin = [&](auto khc) {
      constexpr int KH = decltype(khc)::value;
      if (q == ks) {
#pragma unroll
        for (int i = 0; i < 2; ++i)
#pragma unroll
          for (int ni = 0; ni < 9; ++ni) Y[(i * 9 + ni) * 64 + lane] = acc[2 * KH + i][ni] + X[((wave ^ 2) * 18 + i * 9 + ni) * 64 + lane];
      } else {
#pragma unroll
        for (int i = 0; i < 2; ++i)
#pragma unroll
          for (int ni = 0; ni < 9; ++ni) {
            const f32x4 v = acc[2 * KH + i][ni] + X[((wave ^ 2) * 18 + i * 9 + ni) * 64 + lane];
            __builtin_amdgcn_raw_buffer_store_b128(__builtin_bit_cast(u32x4, v), prs, lane * 16 + (i * 9 + ni) * 1024, base, 16);
          }
      }
    };
    if (kh) fin(std::integral_constant<int, 1>{}); else fin(std::integral_constant<int, 0>{});
  }
  STAMP(2, 3);
  u32x2 xb[3], bb[3];
  int mrow[3], ncol[3];
#pragma unroll
  for (int it = 0; it < 3; ++it) {
    int idx = tid + it * 512; if (idx >= 1152) idx = tid;
    const int fi = idx >> 6, l = idx & 63;
    mrow[it] = pm * 128 + ks * 32 + (fi / 9) * 16 + (l & 15);
    ncol[it] = pn * 144 + (fi % 9) * 16 + 4 * (l >> 4);
    xb[it] = *(const u32x2*)(x + (size_t)mrow[it] * D_ + ncol[it]);
    bb[it] = *(const u32x2*)(b2 + ncol[it]);
  }
  asm volatile("s_waitcnt vmcnt(0)" ::: "memory");
  __syncthreads();
  STAMP(2, 4);
  if (tid == 0) __hip_atomic_store((gu32*)flags + (tile * 4 + ks) * FLAG_STRIDE, 1u, RLX_AGENT);
  if (wave == 7) {
    const int src = lane & 3;
    for (unsigned spins = 0; spins < (1u << 22); ++spins) {
      unsigned v = (src == ks) ? 1u : __hip_atomic_load((gu32*)flags + (tile * 4 + src) * FLAG_STRIDE, RLX_AGENT);
      if (__all(v == 1u)) break;
      __builtin_amdgcn_s_sleep(1);
    }
  }
  __syncthreads();
  STAMP(2, 5);
#pragma unroll
  for (int it = 0; it < 3; ++it) {
    const int idx = tid + it * 512;
    if (idx < 1152) {
      f32x4 p[4];
#pragma unroll
      for (int src = 0; src < 4; ++src) {
        if (src == ks) p[src] = Y[idx];
        else p[src] = __builtin_bit_cast(f32x4, __builtin_amdgcn_raw_buffer_load_b128(prs, idx * 16, ((tile * 4 + ks) * 4 + src) * 18 * 1024, 16));
      }
      const float xv[4] = {lo_f(xb[it][0]), hi_f(xb[it][0]), lo_f(xb[it][1]), hi_f(xb[it][1])};
      const float bv[4] = {lo_f(bb[it][0]), hi_f(bb[it][0]), lo_f(bb[it][1]), hi_f(bb[it][1])};
      float o[4];
#pragma unroll
      for (int i = 0; i < 4; ++i) {
        const float sv = rbf(rbf(p[0][i]) - rbf(p[1][i]));
        const float z = rbf(rbf(p[2][i] + p[3][i]) + bv[i]);
        const float g = rbf(z * __builtin_amdgcn_rcpf(1.0f + __builtin_amdgcn_exp2f(-1.4426950408889634f * z)));
        o[i] = xv[i] + rbf(sv * g);
      }
      u32x2 w = {pk_bf16(o[0], o[1]), pk_bf16(o[2], o[3])};
      *(u32x2*)(out + (size_t)mrow[it] * D_ + ncol[it]) = w;
    }
  }
  STAMP(2, 2);
}

extern "C" void kernel_launch(void* const* d_in, const int* in_sizes, int n_in, void* d_out, int out_size, void* d_ws,
                              size_t ws_size, hipStream_t stream) {
  const u16* x = (const u16*)d_in[0];
  const u16* ws_re = (const u16*)d_in[1];
  const u16* ws_im = (const u16*)d_in[2];
  const u16* wd_re = (const u16*)d_in[3];
  const u16* wd_im = (const u16*)d_in[4];
  const u16* w1 = (const u16*)d_in[6];
  const u16* b1 = (const u16*)d_in[7];
  const u16* w2 = (const u16*)d_in[8];
  const u16* b2 = (const u16*)d_in[9];
  u16* out = (u16*)d_out;
  char* ws = (char*)d_ws;
  const size_t SD = (size_t)S_ * D_;
  u16* xn = (u16*)ws;
  u16* y = xn + SD;
  u16* abT = y + SD;
  u16* h = abT + 2 * SD;
  u16* w2t = h + (size_t)S_ * F_;
  float* part = (float*)(w2t + (size_t)F_ * D_);
  unsigned* flags = (unsigned*)(part + (size_t)64 * 16 * 18 * 256);
  k_ln<<<S_ / 4, 256, 0, stream>>>(x, xn, y, w1, wd_re, wd_im);
  k_gemm1<<<256, 512, 0, stream>>>(xn, y, wd_re, wd_im, w1, b1, abT, h, w2, w2t, flags, ws_re, ws_im);
  k_gemm2<<<256, 512, 0, stream>>>(ws_re, ws_im, abT, h, w2t, part, flags, x, b2, out);
}
```

```cpp
#include <hip/hip_runtime.h>
#include <hip/hip_bf16.h>
#include <stdint.h>
#include <type_traits>

typedef unsigned short u16;
typedef short bf16x8 __attribute__((ext_vector_type(8)));
typedef short s16x4 __attribute__((ext_vector_type(4)));
typedef float f32x4 __attribute__((ext_vector_type(4)));
typedef unsigned u32x4 __attribute__((ext_vector_type(4)));
typedef unsigned u32x2 __attribute__((ext_vector_type(2)));
#define LAS __attribute__((address_space(3)))
#define GAS __attribute__((address_space(1)))
#define DI __device__ __forceinline__

constexpr int S_ = 2048, D_ = 576, F_ = 4608;

#define STAMP(kid, idx) do { } while (0)
#define STAMPL(kid, idx) do { } while (0)
#define STAMPT(T, kid, idx) do { } while (0)
#define STAMPB(kid, blk, idx) do { } while (0)

DI float bf2f(unsigned b) { return __uint_as_float(b << 16); }
typedef __bf16 bf16x2v __attribute__((ext_vector_type(2)));
typedef float f32x2 __attribute__((ext_vector_type(2)));
DI unsigned pk_bf16(float lo, float hi) {
  bf16x2v r = __builtin_convertvector((f32x2){lo, hi}, bf16x2v);
  return __builtin_bit_cast(unsigned, r);
}
DI float rbf(float x) { return __uint_as_float(pk_bf16(x, x) << 16); }
DI float lo_f(unsigned p) { return __uint_as_float(p << 16); }
DI float hi_f(unsigned p) { return __uint_as_float(p & 0xffff0000u); }

template <int N> DI void wait_vm() { asm volatile("s_waitcnt vmcnt(%0)" ::"n"(N) : "memory"); }
DI void raw_barrier() {
  asm volatile("s_waitcnt lgkmcnt(0)" ::: "memory");
  __builtin_amdgcn_s_barrier();
  asm volatile("" ::: "memory");
}

DI void sched_fence() { __builtin_amdgcn_sched_barrier(0); }
typedef unsigned long long u64;
typedef GAS u64 gu64;
#define RLX_AGENT __ATOMIC_RELAXED, __HIP_MEMORY_SCOPE_AGENT

template <int CTRL, int RM> DI float dpp_add(float v) {
  const int t = __builtin_amdgcn_update_dpp(0, __builtin_bit_cast(int, v), CTRL, RM, 0xf, false);
  return v + __builtin_bit_cast(float, t);
}
DI float wave_total(float v) {
  v = dpp_add<0x111, 0xf>(v); v = dpp_add<0x112, 0xf>(v); v = dpp_add<0x114, 0xf>(v); v = dpp_add<0x118, 0xf>(v);
  v = dpp_add<0x142, 0xa>(v); v = dpp_add<0x143, 0xc>(v);
  return __builtin_bit_cast(float, __builtin_amdgcn_readlane(__builtin_bit_cast(int, v), 63));
}
DI void ln_stats(const float (&v)[2][16], bool has2, float (&mb)[2], float (&den)[2]) {
  float s[2], q[2], x0[2];
#pragma unroll
  for (int r = 0; r < 2; ++r) {
    x0[r] = __builtin_bit_cast(float, __builtin_amdgcn_readfirstlane(__builtin_bit_cast(int, v[r][0])));
    float a = 0.f, b = 0.f;
#pragma unroll
    for (int i = 0; i < 8; ++i) { const float d = v[r][i] - x0[r]; a += d; b += d * d; }
    float a2 = 0.f, b2 = 0.f;
#pragma unroll
    for (int i = 8; i < 16; ++i) { const float d = v[r][i] - x0[r]; a2 += d; b2 += d * d; }
    s[r] = has2 ? a + a2 : a; q[r] = has2 ? b + b2 : b;
  }
#pragma unroll
  for (int r = 0; r < 2; ++r) { s[r] = wave_total(s[r]); q[r] = wave_total(q[r]); }
#pragma unroll
  for (int r = 0; r < 2; ++r) {
    const float md = s[r] * (1.0f / D_);
    const float var = rbf(fmaxf(q[r] * (1.0f / D_) - md * md, 0.f));
    mb[r] = rbf(x0[r] + md);
    den[r] = rbf(sqrtf(rbf(var + 1e-5f)));
  }
}

DI int w2_pos(int k) { return (k & ~12) | ((k & 4) << 1) | ((k & 8) >> 1); }
DI void tag_issue(u64& e, const u64* ctrl) { asm volatile("s_load_dwordx2 %0, %1, 0x0" : "=s"(e) : "s"(ctrl)); }
DI void tag_wait(u64& e) { asm volatile("s_waitcnt lgkmcnt(0)" : "+s"(e)); e += 1ull; }

DI void stats_phase(const u16* __restrict__ x, float* st, int row0) {
  const int lane = threadIdx.x & 63;
  const bool has2 = lane < 8;
  u32x4 p0[2], p1[2];
#pragma unroll
  for (int r = 0; r < 2; ++r) {
    const u16* xr = x + (size_t)(row0 + r) * D_;
    p0[r] = *(const u32x4*)(xr + lane * 8);
    p1[r] = *(const u32x4*)(xr + 512 + (lane & 7) * 8);
  }
  float v[2][16];
#pragma unroll
  for (int r = 0; r < 2; ++r)
#pragma unroll
    for (int i = 0; i < 4; ++i) { v[r][2 * i] = lo_f(p0[r][i]); v[r][2 * i + 1] = hi_f(p0[r][i]); v[r][8 + 2 * i] = lo_f(p1[r][i]); v[r][9 + 2 * i] = hi_f(p1[r][i]); }
  float mb1[2], den1[2], mb2[2], den2[2];
  ln_stats(v, has2, mb1, den1);
#pragma unroll
  for (int r = 0; r < 2; ++r) {
    const float inv = 1.0f / den1[r];
#pragma unroll
    for (int i = 0; i < 16; ++i) v[r][i] = (v[r][i] - mb1[r]) * inv;
  }
  ln_stats(v, has2, mb2, den2);
  if (lane < 4) {
    const int r = lane & 1, w = lane >> 1;
    const float m1 = r ? mb1[1] : mb1[0], d1 = r ? den1[1] : den1[0], m2 = r ? mb2[1] : mb2[0], d2 = r ? den2[1] : den2[0];
    const float rr = w ? 1.0f / (d1 * d2) : 1.0f / d1;
    const float cc = w ? m1 + m2 * d1 : m1;
    const u64 pk = ((u64)__float_as_uint(rr * cc) << 32) | __float_as_uint(rr);
    __hip_atomic_store((gu64*)st + w * S_ + row0 + r, pk, RLX_AGENT);
  }
}
template <int PER> DI void wait_flags(const u64* flags, int n, u64 tag) {
  const int lane = threadIdx.x & 63;
  for (unsigned spins = 0; spins < (1u << 21); ++spins) {
    bool ok = true;
#pragma unroll
    for (int i = 0; i < PER; ++i) {
      const int idx = i * 64 + lane;
      const u64 v = __hip_atomic_load((gu64*)flags + (idx < n ? idx : 0), RLX_AGENT);
      ok = ok && (v == tag || idx >= n);
    }
    if (__all(ok)) break;
    __builtin_amdgcn_s_sleep(1);
  }
}

template <int I, int N, class F> DI void static_for(F&& f) {
  if constexpr (I < N) { f(std::integral_constant<int, I>{}); static_for<I + 1, N>(f); }
}
template <int OFF> DI void ds_rd128(bf16x8& d, unsigned addr) {
  asm volatile("ds_read_b128 %0, %1 offset:%2" : "=v"(d) : "v"(addr), "i"(OFF));
}
template <int OFF> DI void ds_rdtr(s16x4& d, unsigned addr) {
  asm volatile("ds_read_b64_tr_b16 %0, %1 offset:%2" : "=v"(d) : "v"(addr), "i"(OFF));
}
DI void glds16(unsigned lds, unsigned voff, const char* sbase) {
  asm volatile("s_mov_b32 m0, %0\n\ts_nop 0\n\tglobal_load_lds_dwordx4 %1, %2" ::"s"(lds), "v"(voff), "s"(sbase) : "memory");
}
template <int LOFF, int GOFF> DI void glds16i(unsigned lds, unsigned voff, unsigned blo, unsigned bhi) {
  asm volatile("s_add_u32 vcc_lo, %2, %5\n\ts_addc_u32 vcc_hi, %3, 0\n\ts_add_u32 m0, %0, %4\n\ts_nop 0\n\tglobal_load_lds_dwordx4 %1, vcc"
               ::"s"(lds), "v"(voff), "s"(blo), "s"(bhi), "i"(LOFF), "i"(GOFF) : "memory", "vcc", "scc");
}
DI void glds16c(unsigned lds, unsigned voff, const char* sbase) {
  asm volatile("s_mov_b32 m0, %0\n\ts_nop 0\n\tglobal_load_lds_dwordx4 %1, %2 sc1" ::"s"(lds), "v"(voff), "s"(sbase) : "memory");
}
DI void glds16c_half(unsigned lds, unsigned voff, const char* sbase) {
  asm volatile("s_mov_b32 m0, %0\n\ts_mov_b32 exec_hi, 0\n\tglobal_load_lds_dwordx4 %1, %2 sc1\n\ts_mov_b32 exec_hi, -1" ::"s"(lds), "v"(voff), "s"(sbase) : "memory");
}
DI void glds16_half(unsigned lds, unsigned voff, const char* sbase) {
  asm volatile("s_mov_b32 m0, %0\n\ts_mov_b32 exec_hi, 0\n\tglobal_load_lds_dwordx4 %1, %2\n\ts_mov_b32 exec_hi, -1" ::"s"(lds), "v"(voff), "s"(sbase) : "memory");
}
DI void mfma_acc(f32x4& acc, const bf16x8& a, const bf16x8& b) {
  asm volatile("v_mfma_f32_16x16x32_bf16 %0, %1, %2, %0" : "+v"(acc) : "v"(a), "v"(b));
}
DI void mfma_new(f32x4& acc, const bf16x8& a, const bf16x8& b) {
  asm volatile("v_mfma_f32_16x16x32_bf16 %0, %1, %2, 0" : "+v"(acc) : "v"(a), "v"(b));
}
DI void mfma_settle() { asm volatile("s_nop 7\n\ts_nop 7" ::: "memory"); }
template <class T> DI void tie(T& v) { asm volatile("" : "+v"(v)); }

DI f32x2 gelu2(f32x2 x) {
  const float c0 = -2.0f * 0.7978845608028654f * 1.4426950408889634f;
  const float c1 = c0 * 0.044715f;
  const f32x2 z = x * (x * x * c1 + c0);
  f32x2 d;
  d.x = __builtin_amdgcn_exp2f(z.x); d.y = __builtin_amdgcn_exp2f(z.y);
  d += 1.0f;
  f32x2 r;
  r.x = __builtin_amdgcn_rcpf(d.x); r.y = __builtin_amdgcn_rcpf(d.y);
  return x * r;
}
DI float gelu_f(float x) {
  const float c0 = -2.0f * 0.7978845608028654f * 1.4426950408889634f;
  const float c1 = c0 * 0.044715f;
  float z = x * __builtin_fmaf(x * x, c1, c0);
  float e = __builtin_amdgcn_exp2f(z);
  return x * __builtin_amdgcn_rcpf(1.0f + e);
}

#define AUX0 2
#define HAUX 16
constexpr unsigned K2_EPI = 4 * 8192;
constexpr int K2_NCH = 20, K2_NK = 9, K2_TOT = 7 * K2_NK;
DI int nn_u(int k) { return ((k >> 1) & 1) | (((k >> 3) & 1) << 1); }
DI unsigned vadd_s(unsigned v, unsigned sc) { unsigned r; asm volatile("v_add_u32 %0, %1, %2" : "=v"(r) : "s"(sc), "v"(v)); return r; }
DI unsigned vxad_s(unsigned v, unsigned sc) { unsigned r; asm volatile("v_xad_u32 %0, %1, 64, %2" : "=v"(r) : "v"(v), "s"(sc)); return r; }
DI void flag_issue(u64& v, const u64* p) { asm volatile("global_load_dwordx2 %0, %1, off sc1" : "=v"(v) : "v"(p)); }
DI void flag_wait(u64& v) { asm volatile("s_waitcnt vmcnt(0)" : "+v"(v)); }
DI void touch1(unsigned& sink, const char* p) { asm volatile("global_load_dword %0, %1, off" : "+v"(sink) : "v"(p)); }
struct K2Aux { const char* ws_re; const char* ws_im; const char* w2; const u16* x; float* st; u64* flags0; const u64* ctrl; const char* e_st; const char* e_bias; int blk; int pm; };

template <bool BNN, bool SWAP, int ROLE>
DI void k2_main(unsigned lds0, int wave, const char* gA, const char* gB, int ldb_b, const K2Aux& aux, f32x4 (&acc)[4][6], u64& tag) {
  const int lane = threadIdx.x & 63, fr = lane & 15, fq = lane >> 4;
  const int wm = wave >> 1, wn = wave & 1;
  unsigned vA[2], vB[2];
  if (ROLE == 0) {
#pragma unroll
    for (int i = 0; i < 2; ++i) {
      const int bb = (wave * 2 + i) * 1024 + lane * 16, r = bb >> 7, c = (bb >> 4) & 7;
      vA[i] = (unsigned)(r * (D_ * 2) + ((c ^ ((r >> 1) & 7)) * 16));
      if (!BNN) vB[i] = (unsigned)(r * ldb_b + ((c ^ ((r >> 1) & 7)) * 16));
      else vB[i] = (unsigned)(r * ldb_b + ((c ^ (2 * nn_u(r))) * 16));
    }
  }
  const unsigned ldsw = lds0 + (unsigned)(wave * 2048);
  const unsigned gAlo = (unsigned)(size_t)gA, gAhi = (unsigned)((size_t)gA >> 32), gBlo = (unsigned)(size_t)gB, gBhi = (unsigned)((size_t)gB >> 32);
  auto issue = [&](auto gc, auto ic) {
    constexpr int G = decltype(gc)::value, I = decltype(ic)::value, ST = G / 7, C = G % 7, SLOT = G % K2_NCH;
    if constexpr (ROLE == 0 && G < K2_TOT) {
      if constexpr (C < 4) glds16i<SLOT * 8192 + I * 1024, C * 64 * D_ * 2 + ST * 128>(ldsw, vA[I], gAlo, gAhi);
      else if constexpr (!BNN) glds16i<SLOT * 8192 + I * 1024, (C - 4) * 64 * (D_ * 2) + ST * 128>(ldsw, vB[I], gBlo, gBhi);
      else glds16i<SLOT * 8192 + I * 1024, ST * 64 * (F_ * 2) + (C - 4) * 128>(ldsw, vB[I], gBlo, gBhi);
    }
  };
  unsigned sink = 0;
  const int t4 = threadIdx.x - 256;
  auto aux_step = [&](auto sc) {
    constexpr int STEP = decltype(sc)::value;
    if constexpr (ROLE == 1) {
      const int li = STEP * (240 * 256) + aux.blk * 256 + t4;
      if (li < 131072 + 41472) {
        const char* p = li < 65536 ? aux.ws_re + (size_t)li * 128 : (li < 131072 ? aux.ws_im + (size_t)(li - 65536) * 128 : aux.w2 + (size_t)(li - 131072) * 128);
        touch1(sink, p);
      }
    }
  };
  unsigned aoff[2], boff[6];
#pragma unroll
  for (int kk = 0; kk < 2; ++kk) aoff[kk] = lds0 + (unsigned)(fr * 128 + (((kk * 4 + fq) ^ ((fr >> 1) & 7)) * 16));
#pragma unroll
  for (int ni = 0; ni < 6; ++ni) {
    const int f4 = (wn * 6 + (ni ^ (wm & 1))) & 3;
    if (!BNN) boff[ni] = lds0 + (unsigned)((f4 * 16 + fr) * 128 + ((fq ^ ((fr >> 1) & 7)) * 16));
    else {
      const int g = lane >> 4, q = (lane & 15) >> 2, p = lane & 3, k = 8 * g + q;
      boff[ni] = lds0 + (unsigned)(k * 128 + ((2 * (f4 ^ nn_u(k)) + (p >> 1)) * 16) + (p & 1) * 8);
    }
  }
  struct Fr { bf16x8 a[4]; bf16x8 b[6]; s16x4 bl[6], bh[6]; };
  Fr f0, f1;
  constexpr int NRD = 4 + (BNN ? 12 : 6);
  unsigned ra, rb[6];
  auto set_addr = [&](auto sc, auto kc) {
    constexpr int ST = decltype(sc)::value, KK = decltype(kc)::value;
    int sa = (7 * ST) % K2_NCH + wm; if (sa >= K2_NCH) sa -= K2_NCH;
    ra = vadd_s(aoff[KK], (unsigned)(sa * 8192));
    constexpr unsigned S0 = ((7 * ST + 4) % K2_NCH) * 8192, S1 = ((7 * ST + 5) % K2_NCH) * 8192, S2 = ((7 * ST + 6) % K2_NCH) * 8192;
    const unsigned lo = wn ? S1 : S0, hi = wn ? S2 : S1, mid = wn ? S2 : S0;
#pragma unroll
    for (int ni = 0; ni < 6; ++ni) {
      const unsigned sb = ni < 2 ? lo : (ni < 4 ? mid : hi);
      if (!BNN && KK == 1) rb[ni] = vxad_s(boff[ni], sb); else rb[ni] = vadd_s(boff[ni], sb);
    }
  };
  auto rd = [&](auto kc, auto jc, Fr& f) {
    constexpr int KK = decltype(kc)::value, J = decltype(jc)::value;
    if constexpr (J < 4) ds_rd128<J * 2048>(f.a[J], ra);
    else if constexpr (!BNN) ds_rd128<0>(f.b[J - 4], rb[J - 4]);
    else {
      constexpr int ni = (J - 4) >> 1, t = (J - 4) & 1;
      if constexpr (t == 0) ds_rdtr<KK * 4096>(f.bl[ni], rb[ni]); else ds_rdtr<KK * 4096 + 512>(f.bh[ni], rb[ni]);
    }
  };
  auto waitf = [&](Fr& f) {
    asm volatile("s_waitcnt lgkmcnt(0)" ::: "memory");
#pragma unroll
    for (int i = 0; i < 4; ++i) tie(f.a[i]);
#pragma unroll
    for (int i = 0; i < 6; ++i) { if (!BNN) tie(f.b[i]); else { tie(f.bl[i]); tie(f.bh[i]); } }
  };
  auto mm = [&](auto ic, const Fr& f) {
    constexpr int I = decltype(ic)::value, mi = I / 6, ni = I % 6;
    bf16x8 bb;
    if constexpr (!BNN) bb = f.b[ni]; else bb = __builtin_shufflevector(f.bl[ni], f.bh[ni], 0, 1, 2, 3, 4, 5, 6, 7);
    if (SWAP) mfma_acc(acc[mi][ni], bb, f.a[mi]); else mfma_acc(acc[mi][ni], f.a[mi], bb);
  };
  f32x4 csa[3] = {(f32x4){0.f, 0.f, 0.f, 0.f}, (f32x4){0.f, 0.f, 0.f, 0.f}, (f32x4){0.f, 0.f, 0.f, 0.f}};
  bf16x8 ones = {0x3f80, 0x3f80, 0x3f80, 0x3f80, 0x3f80, 0x3f80, 0x3f80, 0x3f80};
  auto mcs = [&](auto jc, const Fr& f) {
    constexpr int J = decltype(jc)::value, ni = 2 * J;
    bf16x8 bb;
    if constexpr (!BNN) bb = f.b[ni]; else bb = __builtin_shufflevector(f.bl[ni], f.bh[ni], 0, 1, 2, 3, 4, 5, 6, 7);
    if (SWAP) mfma_acc(csa[J], bb, ones); else mfma_acc(csa[J], ones, bb);
  };
  auto substep = [&](Fr& fc, Fr& fn, auto rsc, auto rkc, auto p0c, auto p1c, auto ckc) {
    constexpr int RS = decltype(rsc)::value, RK = decltype(rkc)::value, P0 = decltype(p0c)::value, P1 = decltype(p1c)::value;
    if constexpr (RS >= 0) set_addr(std::integral_constant<int, (RS >= 0 ? RS : 0)>{}, rkc);
    sched_fence();
    static_for<0, 24>([&](auto ic) {
      constexpr int I = decltype(ic)::value;
      mm(ic, fc);
      if constexpr (RS >= 0 && I < NRD) rd(rkc, ic, fn);
      constexpr int NP = P1 - P0;
      if constexpr (I >= 24 - 2 * NP && ((24 - I) % 2) == 0) {
        constexpr int P = P1 - (24 - I) / 2;
        issue(std::integral_constant<int, P / 2>{}, std::integral_constant<int, P % 2>{});
      }
      sched_fence();
    });
    if constexpr (decltype(ckc)::value == ROLE) static_for<0, 3>([&](auto jc) { mcs(jc, fc); sched_fence(); });
  };
  using IC = std::integral_constant<int, 0>;
  (void)sizeof(IC);
  if constexpr (ROLE == 0) {
    static_for<0, K2_NCH>([&](auto gc) { issue(gc, std::integral_constant<int, 0>{}); issue(gc, std::integral_constant<int, 1>{}); });
    tag_issue(tag, aux.ctrl); tag_wait(tag);
    wait_vm<(K2_NCH - 7) * 2>();
  } else {
    tag_issue(tag, aux.ctrl);
    stats_phase(aux.x, aux.st, aux.blk * 8 + (wave - 4) * 2);
    tag_wait(tag);
  }
#pragma unroll
  for (int i = 0; i < 4; ++i)
#pragma unroll
    for (int j = 0; j < 6; ++j) { acc[i][j] = (f32x4){0.f, 0.f, 0.f, 0.f}; tie(acc[i][j]); }
  tie(ones); tie(csa[0]); tie(csa[1]); tie(csa[2]);
  raw_barrier();
  STAMP(1, 3);
  u64 fl = 0;
  set_addr(std::integral_constant<int, 0>{}, std::integral_constant<int, 0>{});
  static_for<0, NRD>([&](auto jc) { rd(std::integral_constant<int, 0>{}, jc, f0); });
  static_for<0, K2_NK>([&](auto sc) {
    constexpr int K = decltype(sc)::value;
    constexpr int PB = 2 * (7 * (K - 1) + K2_NCH);
    waitf(f0);
    if constexpr (K >= 1) substep(f0, f1, sc, std::integral_constant<int, 1>{}, std::integral_constant<int, PB + 7>{}, std::integral_constant<int, PB + 14>{}, std::integral_constant<int, 0>{});
    else substep(f0, f1, sc, std::integral_constant<int, 1>{}, std::integral_constant<int, 0>{}, std::integral_constant<int, 0>{}, std::integral_constant<int, 0>{});
    waitf(f1);
    if constexpr (K < K2_NK - 1) {
      constexpr int ISS = (7 * K + K2_NCH - 1 < K2_TOT - 1) ? 7 * K + K2_NCH - 1 : K2_TOT - 1;
      if (ROLE == 0) wait_vm<(ISS - (7 * K + 13)) * 2>();
      if constexpr (ROLE == 1 && (K == 1 || K == 7)) wait_vm<0>();
      __builtin_amdgcn_s_barrier();
      asm volatile("" ::: "memory");
      if constexpr (ROLE == 1 && K == 1) { if (threadIdx.x == 256) __hip_atomic_store((gu64*)aux.flags0 + aux.blk, tag, RLX_AGENT); }
      if constexpr (ROLE == 1 && K == 5) {
        if (wave == 7) flag_issue(fl, aux.flags0 + aux.pm * 32 + (lane & 31));
      }
      if constexpr (ROLE == 1 && K == 6) {
        if (wave == 7) {
          flag_wait(fl);
          if (!__all(fl == tag)) wait_flags<1>(aux.flags0 + aux.pm * 32, 32, tag);
          const unsigned eo = lds0 + K2_EPI;
          glds16c(eo, (unsigned)(lane * 16), aux.e_st);
          glds16c(eo + 1024u, (unsigned)(1024 + lane * 16), aux.e_st);
          glds16(eo + 3072u, (unsigned)((lane < 24 ? lane : 0) * 16), aux.e_bias);
        }
      }
      if constexpr (K >= AUX0 && K < AUX0 + 3) aux_step(std::integral_constant<int, K - AUX0>{});
      constexpr int PC = 2 * (7 * K + K2_NCH);
      substep(f1, f0, std::integral_constant<int, K + 1>{}, std::integral_constant<int, 0>{}, std::integral_constant<int, PC>{}, std::integral_constant<int, PC + 7>{}, std::integral_constant<int, 1>{});
    } else {
      substep(f1, f0, std::integral_constant<int, -1>{}, std::integral_constant<int, 0>{}, std::integral_constant<int, 0>{}, std::integral_constant<int, 0>{}, std::integral_constant<int, 1>{});
    }
  });
  mfma_settle();
  if (ROLE == 1) { asm volatile("s_waitcnt vmcnt(0)" : "+v"(sink)); }
  {
    LAS float* tb = (LAS float*)(size_t)(lds0 + K2_EPI + 4096u + ROLE * 768u);
#pragma unroll
    for (int jj = 0; jj < 3; ++jj) {
      const int c0 = wn * 96 + ((2 * jj) ^ (wm & 1)) * 16;
      if (SWAP) { if (fr == 0) *(LAS f32x4*)(tb + c0 + 4 * fq) = csa[jj]; }
      else { if (fq == 0) tb[c0 + fr] = csa[jj][0]; }
    }
  }
  raw_barrier();
}

#define FL_G 5
constexpr int K3_NS = 4, K3_A = 16384, K3_B = 18432, K3_STAGE = K3_A + K3_B, K3_CNT = 9, K3_S = 16, K3_O = 18, K3_N = K3_S + K3_O;
constexpr unsigned K3_SP = 2 * K3_STAGE;
constexpr unsigned K3_SOWN = 4 * K3_STAGE;

struct K3Ctx {
  const char* sA; const char* sB; const char* oA; const char* oB;
  char* sp;
  u64* ctrl; u64* flagsA; u64* flags1; u64* flagsS;
  int blk, tile, q, rs, ro;
};

DI void k3_loader(unsigned lds0, int lw, const K3Ctx& c, u64 tag) {
  int tz = threadIdx.x; tie(tz);
  const int lane = tz & 63;
  constexpr int NS = K3_NS, STAGE = K3_STAGE, CNT = K3_CNT;
  unsigned vAs[4], vAo[4], vBs[5], vBo[5];
#pragma unroll
  for (int i = 0; i < 4; ++i) {
    const int b = (lw * 4 + i) * 1024 + lane * 16, r = b >> 7, cc = (b >> 4) & 7;
    vAs[i] = (unsigned)(r * (S_ * 2) + ((cc ^ ((r >> 1) & 7)) * 16));
    vAo[i] = (unsigned)(r * (F_ * 2) + ((cc ^ ((r >> 1) & 7)) * 16));
  }
#pragma unroll
  for (int i = 0; i < 5; ++i) {
    const int b = lw * 4608 + i * 1024 + ((i < 4) ? lane : (lane & 31)) * 16, r = b >> 7, cc = (b >> 4) & 7;
    vBs[i] = (unsigned)(r * (S_ * 2) + ((cc ^ ((r >> 1) & 7)) * 16));
    vBo[i] = (unsigned)(w2_pos(b / 288) * (D_ * 2) + (b % 288));
  }
  const unsigned la = lds0 + (unsigned)(lw * 4096), lb = lds0 + (unsigned)(K3_A + lw * 4608);
  auto stage_s = [&](int g, bool doA, bool doB) {
    const unsigned slot = (unsigned)((g & 3) * STAGE);
    const int kt = (g + c.rs) & 15;
    if (doA) {
#pragma unroll
      for (int i = 0; i < 4; ++i) glds16(la + slot + (unsigned)(i * 1024), vAs[i], c.sA + kt * 128);
    }
    if (doB) {
#pragma unroll
      for (int i = 0; i < 4; ++i) glds16c(lb + slot + (unsigned)(i * 1024), vBs[i], c.sB + kt * 128);
      glds16c_half(lb + slot + 4096u, vBs[4], c.sB + kt * 128);
    }
  };
  auto stage_o = [&](int g) {
    const unsigned slot = (unsigned)((g & 3) * STAGE);
    int kt = g - K3_S + c.ro; if (kt >= K3_O) kt -= K3_O;
#pragma unroll
    for (int i = 0; i < 4; ++i) glds16c(la + slot + (unsigned)(i * 1024), vAo[i], c.oA + kt * 128);
#pragma unroll
    for (int i = 0; i < 4; ++i) glds16(lb + slot + (unsigned)(i * 1024), vBo[i], c.oB + (long)kt * (64 * D_ * 2));
    glds16_half(lb + slot + 4096u, vBo[4], c.oB + (long)kt * (64 * D_ * 2));
  };
#pragma unroll
  for (int g = 0; g < NS; ++g) stage_s(g, true, false);
  if (lw == 0) wait_flags<1>(c.flagsA, 48, tag);
  STAMPL(3, 3);
  raw_barrier();
#pragma unroll
  for (int g = 0; g < NS; ++g) stage_s(g, false, true);
  wait_vm<15>();
  raw_barrier();
  wait_vm<10>(); raw_barrier(); stage_s(4, true, true);
  wait_vm<5 + CNT>(); raw_barrier(); stage_s(5, true, true);
  for (int k = 2; k < K3_N - NS; ++k) {
    wait_vm<2 * CNT>();
    if (k == K3_S - NS && lw == 0) {
      STAMPL(3, 0);
      wait_flags<4>(c.flags1, 256, tag);
      STAMPL(3, 1);
      if (c.blk == 0 && lane == 0) __hip_atomic_store((gu64*)c.ctrl, tag, RLX_AGENT);
    }
    raw_barrier();
    if (k + NS < K3_S) stage_s(k + NS, true, true); else stage_o(k + NS);
    if (k == FL_G && tz == 256) { __hip_atomic_store((gu64*)c.flags1 + c.blk, tag, RLX_AGENT); STAMPL(3, 2); }
    if (k == K3_S + 3 && tz == 256) __hip_atomic_store((gu64*)c.flagsS + c.tile * 4 + c.q, tag, RLX_AGENT);
  }
  const unsigned vl = (unsigned)(lane * 16);
  wait_vm<2 * CNT>();
  if (lw == 0) wait_flags<1>(c.flagsS + c.tile * 4, 4, tag);
  raw_barrier();
  wait_vm<CNT>();
  if (lw < 3) {
    const char* spsrc = c.sp + (size_t)((c.tile * 4 + c.q) * 4 + ((c.q + 1 + lw) & 3)) * 9216;
#pragma unroll
    for (int i = 0; i < 9; ++i) glds16c(lds0 + K3_SP + (unsigned)(lw * 9216 + i * 1024), vl, spsrc + i * 1024);
    raw_barrier();
    wait_vm<9>();
  } else {
    raw_barrier();
    wait_vm<0>();
  }
  raw_barrier();
  wait_vm<0>();
}

DI void k3_compute(unsigned lds0, int wave, const K3Ctx& c, f32x4 (&acc)[2][9]) {
  int tz = threadIdx.x; tie(tz);
  const int lane = tz & 63;
  constexpr int NS = K3_NS, STAGE = K3_STAGE;
  const int fr = lane & 15, fq = lane >> 4;
  const unsigned aoff = lds0 + (unsigned)((wave * 32 + fr) * 128 + ((fq ^ ((fr >> 1) & 7)) * 16));
  const unsigned boffT = lds0 + (unsigned)(K3_A + fr * 128 + ((fq ^ ((fr >> 1) & 7)) * 16));
  unsigned boffN;
  {
    const int g = lane >> 4, q = (lane & 15) >> 2, p = lane & 3;
    boffN = lds0 + (unsigned)(K3_A + ((g >> 1) * 16 + (g & 1) * 4 + q) * 288 + (p >> 1) * 16 + (p & 1) * 8);
  }
  const auto srs = __builtin_amdgcn_make_buffer_rsrc(c.sp, 0, 64 * 16 * 9216, 0x00020000);
  const unsigned vl = (unsigned)(lane * 16);
  struct Fr { bf16x8 a[2]; bf16x8 b[9]; s16x4 bl[9], bh[9]; };
  Fr f0, f1;
  auto rd = [&](auto nn, auto kc, auto jc, Fr& f, unsigned ad, unsigned bd) {
    constexpr int J = decltype(jc)::value, KK = decltype(kc)::value;
    if constexpr (J < 2) ds_rd128<J * 2048>(f.a[J], ad);
    else if constexpr (!decltype(nn)::value) ds_rd128<(J - 2) * 2048>(f.b[J - 2], bd);
    else { ds_rdtr<KK * 32 * 288 + (J - 2) * 32>(f.bl[J - 2], bd); ds_rdtr<KK * 32 * 288 + (J - 2) * 32 + 8 * 288>(f.bh[J - 2], bd); }
  };
  auto waitf = [&](auto nn, Fr& f) {
    asm volatile("s_waitcnt lgkmcnt(0)" ::: "memory");
    tie(f.a[0]); tie(f.a[1]);
#pragma unroll
    for (int i = 0; i < 9; ++i) { if (!decltype(nn)::value) tie(f.b[i]); else { tie(f.bl[i]); tie(f.bh[i]); } }
  };
  auto mm = [&](auto nn, auto pub, auto ic, const Fr& f) {
    constexpr int I = decltype(ic)::value, ni = I / 2, mi = I % 2;
    bf16x8 bb;
    if constexpr (!decltype(nn)::value) bb = f.b[ni]; else bb = __builtin_shufflevector(f.bl[ni], f.bh[ni], 0, 1, 2, 3, 4, 5, 6, 7);
    if constexpr (decltype(pub)::value) {
      if constexpr (mi == 0) {
        if (wave == c.q) {
          *(LAS f32x4*)(size_t)(lds0 + K3_SOWN + ni * 1024 + vl) = acc[0][ni];
          *(LAS f32x4*)(size_t)(lds0 + K3_SOWN + (9 + ni) * 1024 + vl) = acc[1][ni];
        } else {
          const u32x4 w = {pk_bf16(acc[0][ni][0], acc[0][ni][1]), pk_bf16(acc[0][ni][2], acc[0][ni][3]),
                           pk_bf16(acc[1][ni][0], acc[1][ni][1]), pk_bf16(acc[1][ni][2], acc[1][ni][3])};
          __builtin_amdgcn_raw_buffer_store_b128(w, srs, vl + ni * 1024, ((c.tile * 4 + wave) * 4 + c.q) * 9216, 16);
        }
        asm volatile("s_nop 1" ::: "memory");
      }
      mfma_new(acc[mi][ni], bb, f.a[mi]);
    } else mfma_acc(acc[mi][ni], bb, f.a[mi]);
  };
  auto sub = [&](auto cn, auto pub, Fr& fc, Fr& fn, auto rnc, auto rkc, unsigned rslot) {
    constexpr int RN = decltype(rnc)::value, RK = decltype(rkc)::value;
    using NXT = std::integral_constant<bool, RN == 1>;
    unsigned ad = 0, bd = 0;
    if constexpr (RN >= 0) {
      ad = RK ? vxad_s(aoff, rslot) : vadd_s(aoff, rslot);
      if constexpr (RN == 1) bd = vadd_s(boffN, rslot); else bd = RK ? vxad_s(boffT, rslot) : vadd_s(boffT, rslot);
    }
    static_for<0, 18>([&](auto ic) {
      constexpr int I = decltype(ic)::value;
      mm(cn, pub, ic, fc);
      if constexpr (RN >= 0 && I < 11) rd(NXT{}, rkc, ic, fn, ad, bd);
    });
  };
  using F = std::false_type; using T = std::true_type;
  using N0 = std::integral_constant<int, 0>; using N1 = std::integral_constant<int, 1>; using NL = std::integral_constant<int, -1>;
  auto body = [&](auto cn, auto nxc, auto pub, unsigned cslot, unsigned nslot, int ev) {
    constexpr int NX = decltype(nxc)::value;
    waitf(cn, f0);
    sub(cn, pub, f0, f1, std::integral_constant<int, decltype(cn)::value ? 1 : 0>{}, N1{}, cslot);
    waitf(cn, f1);
    if (ev) wait_vm<0>();
    if constexpr (NX >= 0) { __builtin_amdgcn_s_barrier(); asm volatile("" ::: "memory"); }
    sub(cn, F{}, f1, f0, nxc, N0{}, nslot);
  };
#pragma unroll
  for (int i = 0; i < 2; ++i)
#pragma unroll
    for (int jj = 0; jj < 9; ++jj) { acc[i][jj] = (f32x4){0.f, 0.f, 0.f, 0.f}; tie(acc[i][jj]); }
  raw_barrier();
  raw_barrier();
  static_for<0, 11>([&](auto jc) { rd(F{}, N0{}, jc, f0, aoff, boffT); });
  unsigned cslot = 0, nslot = STAGE;
  auto adv = [&]() { cslot = nslot; nslot += STAGE; if (nslot == NS * STAGE) nslot = 0; };
  for (int g = 0; g < K3_S - 1; ++g) { body(F{}, N0{}, F{}, cslot, nslot, g == FL_G ? 1 : 0); adv(); }
  body(F{}, N1{}, F{}, cslot, nslot, 0); adv();
  mfma_settle();
  body(T{}, N1{}, T{}, cslot, nslot, 0); adv();
  for (int g = K3_S + 1; g < K3_N - 1; ++g) { body(T{}, N1{}, F{}, cslot, nslot, g == K3_S + 3 ? 1 : 0); adv(); }
  body(T{}, NL{}, F{}, cslot, 0u, 0);
  mfma_settle();
}

constexpr size_t SD2 = (size_t)S_ * D_ * 2;
constexpr size_t WS_ST = 0, WS_CTRL = 65536, WS_ABT = 131072, WS_H = WS_ABT + 2 * SD2,
                 WS_SP = WS_H + (size_t)S_ * F_ * 2, WS_OP = WS_SP + (size_t)64 * 16 * 9216;

__global__ __launch_bounds__(512, 2) void k_all(const u16* __restrict__ x, const u16* __restrict__ ws_re, const u16* __restrict__ ws_im,
                                                const u16* __restrict__ wd_re, const u16* __restrict__ wd_im,
                                                const u16* __restrict__ w1, const u16* __restrict__ b1,
                                                const u16* __restrict__ w2, const u16* __restrict__ b2,
                                                u16* __restrict__ out, char* ws) {
  __shared__ __attribute__((aligned(1024))) char smem_[K2_NCH * 8192];
  LAS char* smem = (LAS char*)smem_;
  STAMP(1, 0);
  const int b = blockIdx.x, tid = threadIdx.x;
  const int lane = tid & 63, fr = lane & 15, fq = lane >> 4;
  const int wave = __builtin_amdgcn_readfirstlane(tid >> 6);
  const unsigned lds0 = (unsigned)(size_t)smem;
  float* st = (float*)(ws + WS_ST);
  u16* abT = (u16*)(ws + WS_ABT);
  u16* h = (u16*)(ws + WS_H);
  u64* ctrl = (u64*)(ws + WS_CTRL);
  u64* flags0 = ctrl + 512; u64* flagsA = ctrl + 1024; u64* flags1 = ctrl + 1536; u64* flagsS = ctrl + 2048; u64* flags2 = ctrl + 2560;
  u64 tag;

  if (b >= 240) {
    tag_issue(tag, ctrl);
    if (wave >= 4) stats_phase(x, st, b * 8 + (wave - 4) * 2);
    tag_wait(tag);
    wait_vm<0>();
    raw_barrier();
    if (tid == 256) __hip_atomic_store((gu64*)flags0 + b, tag, RLX_AGENT);
  } else {
    const int x8 = b & 7, j8 = b >> 3;
    const int u = j8 < 24 ? j8 : (x8 >> 1) * 6 + (j8 - 24);
    const int pm = (x8 & 1) * 4 + (u & 3), pn = j8 < 24 ? 6 + (x8 >> 1) * 6 + (u >> 2) : (u >> 2);
    const int t = pn * 8 + pm;
    const int wm = wave >> 1, wn = wave & 1, par = wm & 1;
    K2Aux aux{(const char*)ws_re, (const char*)ws_im, (const char*)w2, x, st, flags0, ctrl,
              (const char*)(st + ((size_t)(pn < 6 ? 0 : 1) * S_ + pm * 256) * 2),
              (const char*)(b1 + (pn < 6 ? 0 : (pn - 6) * 192)), b, pm};
    const LAS char* epi = smem + K2_EPI;
    const char* gA = (const char*)(x + (size_t)pm * 256 * D_);
    f32x4 acc[4][6];
    if (pn < 6) {
      const u16* wd = pn < 3 ? wd_re : wd_im;
      const int n0 = (pn < 3 ? pn : pn - 3) * 192;
      const char* gB = (const char*)(wd + (size_t)n0 * D_);
      if (wave < 4) k2_main<false, false, 0>(lds0, wave, gA, gB, D_ * 2, aux, acc, tag);
      else k2_main<false, false, 1>(lds0, wave, gA, gB, D_ * 2, aux, acc, tag);
      STAMP(1, 1);
      u16* dst = abT + (size_t)(pn < 3 ? 0 : 1) * D_ * S_;
      const auto ars = __builtin_amdgcn_make_buffer_rsrc(dst, 0, D_ * S_ * 2, 0x00020000);
#pragma unroll
      for (int mi = 0; mi < 4; ++mi) {
        const f32x4 s01 = *(const LAS f32x4*)(epi + (wm * 64 + mi * 16 + 4 * fq) * 8), s23 = *(const LAS f32x4*)(epi + (wm * 64 + mi * 16 + 4 * fq) * 8 + 16);
#pragma unroll
        for (int ni = 0; ni < 6; ++ni) {
          const int cl = wn * 96 + (ni ^ par) * 16 + fr;
          const float c = *(const LAS float*)(epi + 4096 + cl * 4) + *(const LAS float*)(epi + 4096 + 768 + cl * 4);
          acc[mi][ni][0] = acc[mi][ni][0] * s01[0] - s01[1] * c; acc[mi][ni][1] = acc[mi][ni][1] * s01[2] - s01[3] * c;
          acc[mi][ni][2] = acc[mi][ni][2] * s23[0] - s23[1] * c; acc[mi][ni][3] = acc[mi][ni][3] * s23[2] - s23[3] * c;
        }
      }
#pragma unroll
      for (int ni = 0; ni < 6; ++ni) {
        const int e = n0 + wn * 96 + (ni ^ par) * 16 + fr;
#pragma unroll
        for (int mp = 0; mp < 2; ++mp) {
          unsigned x0 = pk_bf16(acc[2 * mp][ni][0], acc[2 * mp][ni][1]), x1 = pk_bf16(acc[2 * mp][ni][2], acc[2 * mp][ni][3]);
          unsigned y0 = pk_bf16(acc[2 * mp + 1][ni][0], acc[2 * mp + 1][ni][1]), y1 = pk_bf16(acc[2 * mp + 1][ni][2], acc[2 * mp + 1][ni][3]);
          auto r0 = __builtin_amdgcn_permlane16_swap(x0, y0, false, false);
          auto r1 = __builtin_amdgcn_permlane16_swap(x1, y1, false, false);
          u32x4 w = {r0[0], r1[0], r0[1], r1[1]};
          const int s = pm * 256 + wm * 64 + (2 * mp + (fq & 1)) * 16 + (fq >> 1) * 8;
          __builtin_amdgcn_raw_buffer_store_b128(w, ars, (e * S_ + s) * 2, 0, 16);
        }
      }
      STAMPT(0, 0, 3); STAMPT(448, 0, 4);
      wait_vm<0>();
      raw_barrier();
      if (tid == 0) __hip_atomic_store((gu64*)flagsA + t, tag, RLX_AGENT);
      STAMPT(0, 0, 5);
    } else {
      const int n0 = (pn - 6) * 192;
      const char* gB = (const char*)(w1 + n0);
      if (wave < 4) k2_main<true, true, 0>(lds0, wave, gA, gB, F_ * 2, aux, acc, tag);
      else k2_main<true, true, 1>(lds0, wave, gA, gB, F_ * 2, aux, acc, tag);
      STAMP(1, 1);
      const auto hrs = __builtin_amdgcn_make_buffer_rsrc(h, 0, F_ * S_ * 2, 0x00020000);
      float bias[6][4];
      f32x4 csv[6];
      u32x4 hw[4][3];
#pragma unroll
      for (int ni = 0; ni < 6; ++ni) {
        const int cl = wn * 96 + (ni ^ par) * 16 + 4 * fq;
        const u32x2 p = *(const LAS u32x2*)(epi + 3072 + cl * 2);
        bias[ni][0] = lo_f(p[0]); bias[ni][1] = hi_f(p[0]); bias[ni][2] = lo_f(p[1]); bias[ni][3] = hi_f(p[1]);
        csv[ni] = *(const LAS f32x4*)(epi + 4096 + cl * 4) + *(const LAS f32x4*)(epi + 4096 + 768 + cl * 4);
      }
#pragma unroll
      for (int mi = 0; mi < 4; ++mi) {
        const f32x2 sr = *(const LAS f32x2*)(epi + (wm * 64 + mi * 16 + fr) * 8);
#pragma unroll
        for (int np = 0; np < 3; ++np) {
          unsigned pk[2][2];
#pragma unroll
          for (int o = 0; o < 2; ++o) {
            const int ni = 2 * np + o;
            const f32x2 t01 = (f32x2){bias[ni][0], bias[ni][1]} - (f32x2){csv[ni][0], csv[ni][1]} * sr[1];
            const f32x2 t23 = (f32x2){bias[ni][2], bias[ni][3]} - (f32x2){csv[ni][2], csv[ni][3]} * sr[1];
            const f32x2 g01 = gelu2((f32x2){acc[mi][ni][0], acc[mi][ni][1]} * sr[0] + t01);
            const f32x2 g23 = gelu2((f32x2){acc[mi][ni][2], acc[mi][ni][3]} * sr[0] + t23);
            pk[o][0] = pk_bf16(g01.x, g01.y); pk[o][1] = pk_bf16(g23.x, g23.y);
          }
          auto r0 = __builtin_amdgcn_permlane16_swap(pk[0][0], pk[1][0], false, false);
          auto r1 = __builtin_amdgcn_permlane16_swap(pk[0][1], pk[1][1], false, false);
          hw[mi][np] = (u32x4){r0[0], r1[0], r0[1], r1[1]};
        }
      }
      sched_fence();
      STAMPT(0, 0, 0); STAMPT(448, 0, 1);
      sched_fence();
#pragma unroll
      for (int mi = 0; mi < 4; ++mi)
#pragma unroll
        for (int np = 0; np < 3; ++np) {
          const int m = pm * 256 + wm * 64 + mi * 16 + fr;
          const int f = n0 + wn * 96 + ((2 * np + (fq & 1)) ^ par) * 16 + (fq >> 1) * 8;
          __builtin_amdgcn_raw_buffer_store_b128(hw[mi][np], hrs, (m * F_ + f) * 2, 0, HAUX);
        }
      STAMPT(448, 0, 2);
    }
  }
  STAMP(1, 2);
  raw_barrier();

  const int xcd = b & 7, j3 = b >> 3;
  const int q = xcd >> 1, pm3 = (xcd & 1) * 8 + (j3 >> 2), pn3 = j3 & 3;
  const int tile = pm3 * 4 + pn3;
  K3Ctx c;
  c.sA = (const char*)((q < 2 ? ws_re : ws_im) + (size_t)pm3 * 128 * S_ + (q & 1) * 1024);
  c.sB = (const char*)(abT + (size_t)(q >> 1) * D_ * S_ + (size_t)pn3 * 144 * S_ + (q & 1) * 1024);
  c.oA = (const char*)(h + (size_t)pm3 * 128 * F_ + q * 1152);
  c.oB = (const char*)(w2 + (size_t)q * 1152 * D_ + pn3 * 144);
  c.sp = ws + WS_SP;
  c.ctrl = ctrl; c.flagsA = flagsA; c.flags1 = flags1; c.flagsS = flagsS;
  c.blk = b; c.tile = tile; c.q = q; c.rs = (xcd & 1) * 8; c.ro = (xcd & 1) * 9;
  f32x4 acc[2][9];
  STAMP(2, 0);
  if (wave >= 4) k3_loader(lds0, wave - 4, c, tag);
  else k3_compute(lds0, wave, c, acc);
  STAMP(2, 1);
  int tz = threadIdx.x; tie(tz);
  const int lz = tz & 63;
  const auto prs = __builtin_amdgcn_make_buffer_rsrc(ws + WS_OP, 0, 64 * 16 * 9216, 0x00020000);
  LAS f32x4* Y = (LAS f32x4*)(smem + 3 * K3_STAGE);
  if (wave < 4) {
    if (wave == q) {
#pragma unroll
      for (int i = 0; i < 18; ++i) Y[i * 64 + lz] = acc[i / 9][i % 9];
    } else {
#pragma unroll
      for (int i = 0; i < 9; ++i) {
        const u32x4 w = {pk_bf16(acc[0][i][0], acc[0][i][1]), pk_bf16(acc[0][i][2], acc[0][i][3]), pk_bf16(acc[1][i][0], acc[1][i][1]), pk_bf16(acc[1][i][2], acc[1][i][3])};
        __builtin_amdgcn_raw_buffer_store_b128(w, prs, lz * 16 + i * 1024, ((tile * 4 + wave) * 4 + q) * 9216, 16);
      }
    }
  }
  STAMP(2, 3);
  asm volatile("s_waitcnt vmcnt(0)" ::: "memory");
  raw_barrier();
  STAMP(2, 4);
  if (tz == 0) __hip_atomic_store((gu64*)flags2 + tile * 4 + q, tag, RLX_AGENT);
  u32x2 xb[3], bb[3];
  int ooff[3];
  float sv[3][4];
#pragma unroll
  for (int it = 0; it < 3; ++it) {
    int idx = tz + it * 512; if (idx >= 1152) idx = tz;
    const int fi = idx >> 6, l = idx & 63;
    const int mrow = pm3 * 128 + q * 32 + (fi / 9) * 16 + (l & 15);
    const int ncol = pn3 * 144 + (fi % 9) * 16 + 4 * (l >> 4);
    ooff[it] = mrow * D_ + ncol;
    if (wave != 7) { xb[it] = *(const u32x2*)(x + ooff[it]); bb[it] = *(const u32x2*)(b2 + ncol); }
    const f32x4 own = *(const LAS f32x4*)(smem + K3_SOWN + idx * 16);
    f32x4 re = q < 2 ? own : (f32x4){0.f, 0.f, 0.f, 0.f}, im = q < 2 ? (f32x4){0.f, 0.f, 0.f, 0.f} : own;
#pragma unroll
    for (int jj = 0; jj < 3; ++jj) {
      const u32x2 a = *(const LAS u32x2*)(smem + K3_SP + jj * 9216 + ((fi % 9) * 64 + l) * 16 + (fi / 9) * 8);
      const f32x4 v = {lo_f(a[0]), hi_f(a[0]), lo_f(a[1]), hi_f(a[1])};
      if (((q + 1 + jj) & 3) < 2) re += v; else im += v;
    }
#pragma unroll
    for (int i = 0; i < 4; ++i) sv[it][i] = rbf(rbf(re[i]) - rbf(im[i]));
  }
  if (wave == 7) {
    wait_flags<1>(flags2 + tile * 4, 4, tag);
#pragma unroll
    for (int it = 0; it < 3; ++it) { xb[it] = *(const u32x2*)(x + ooff[it]); bb[it] = *(const u32x2*)(b2 + ooff[it] % D_); }
  }
  raw_barrier();
  STAMP(2, 5);
  u32x2 p[3][3];
#pragma unroll
  for (int it = 0; it < 3; ++it) {
    int idx = tz + it * 512; if (idx >= 1152) idx = tz;
    const int fi = idx >> 6, l = idx & 63;
#pragma unroll
    for (int jj = 0; jj < 3; ++jj) {
      const int sw = (q + 1 + jj) & 3;
      p[it][jj] = __builtin_bit_cast(u32x2, __builtin_amdgcn_raw_buffer_load_b64(prs, ((fi % 9) * 64 + l) * 16 + (fi / 9) * 8, ((tile * 4 + q) * 4 + sw) * 9216, 16));
    }
  }
#pragma unroll
  for (int it = 0; it < 3; ++it) {
    const int idx = tz + it * 512;
    if (idx < 1152) {
      const f32x4 own = Y[idx];
      const float xv[4] = {lo_f(xb[it][0]), hi_f(xb[it][0]), lo_f(xb[it][1]), hi_f(xb[it][1])};
      const float bv[4] = {lo_f(bb[it][0]), hi_f(bb[it][0]), lo_f(bb[it][1]), hi_f(bb[it][1])};
      float ov[4] = {own[0], own[1], own[2], own[3]};
#pragma unroll
      for (int jj = 0; jj < 3; ++jj) { ov[0] += lo_f(p[it][jj][0]); ov[1] += hi_f(p[it][jj][0]); ov[2] += lo_f(p[it][jj][1]); ov[3] += hi_f(p[it][jj][1]); }
      float o[4];
#pragma unroll
      for (int i = 0; i < 4; ++i) {
        const float z = rbf(rbf(ov[i]) + bv[i]);
        const float g = rbf(z * __builtin_amdgcn_rcpf(1.0f + __builtin_amdgcn_exp2f(-1.4426950408889634f * z)));
        o[i] = xv[i] + rbf(sv[it][i] * g);
      }
      u32x2 w = {pk_bf16(o[0], o[1]), pk_bf16(o[2], o[3])};
      *(u32x2*)(out + ooff[it]) = w;
    }
  }
  STAMP(2, 2);
}

extern "C" void kernel_launch(void* const* d_in, const int* in_sizes, int n_in, void* d_out, int out_size, void* d_ws,
                              size_t ws_size, hipStream_t stream) {
  k_all<<<256, 512, 0, stream>>>((const u16*)d_in[0], (const u16*)d_in[1], (const u16*)d_in[2], (const u16*)d_in[3], (const u16*)d_in[4],
                                 (const u16*)d_in[6], (const u16*)d_in[7], (const u16*)d_in[8], (const u16*)d_in[9], (u16*)d_out, (char*)d_ws);
}
```

```cpp
#include <hip/hip_runtime.h>
#include <hip/hip_bf16.h>
#include <stdint.h>
#include <type_traits>


typedef unsigned short u16;
typedef short bf16x8 __attribute__((ext_vector_type(8)));
typedef short s16x4 __attribute__((ext_vector_type(4)));
typedef float f32x4 __attribute__((ext_vector_type(4)));
typedef unsigned u32x4 __attribute__((ext_vector_type(4)));
typedef unsigned u32x2 __attribute__((ext_vector_type(2)));
#define LAS __attribute__((address_space(3)))
#define GAS __attribute__((address_space(1)))
#define DI __device__ __forceinline__

constexpr int S_ = 2048, D_ = 576, F_ = 4608;

typedef __bf16 bf16x2v __attribute__((ext_vector_type(2)));
typedef float f32x2 __attribute__((ext_vector_type(2)));
DI unsigned pk_bf16(float lo, float hi) {
  bf16x2v r = __builtin_convertvector((f32x2){lo, hi}, bf16x2v);
  return __builtin_bit_cast(unsigned, r);
}
DI float rbf(float x) { return __uint_as_float(pk_bf16(x, x) << 16); }
DI float lo_f(unsigned p) { return __uint_as_float(p << 16); }
DI float hi_f(unsigned p) { return __uint_as_float(p & 0xffff0000u); }

template <int N> DI void wait_vm() { asm volatile("s_waitcnt vmcnt(%0)" ::"n"(N) : "memory"); }
DI void raw_barrier() {
  asm volatile("s_waitcnt lgkmcnt(0)" ::: "memory");
  __builtin_amdgcn_s_barrier();
  asm volatile("" ::: "memory");
}

DI void sched_fence() { __builtin_amdgcn_sched_barrier(0); }
typedef unsigned long long u64;
typedef GAS u64 gu64;
#define RLX_AGENT __ATOMIC_RELAXED, __HIP_MEMORY_SCOPE_AGENT

template <int CTRL, int RM> DI float dpp_add(float v) {
  const int t = __builtin_amdgcn_update_dpp(0, __builtin_bit_cast(int, v), CTRL, RM, 0xf, false);
  return v + __builtin_bit_cast(float, t);
}
DI float wave_total(float v) {
  v = dpp_add<0x111, 0xf>(v); v = dpp_add<0x112, 0xf>(v); v = dpp_add<0x114, 0xf>(v); v = dpp_add<0x118, 0xf>(v);
  v = dpp_add<0x142, 0xa>(v); v = dpp_add<0x143, 0xc>(v);
  return __builtin_bit_cast(float, __builtin_amdgcn_readlane(__builtin_bit_cast(int, v), 63));
}
DI void ln_stats(const float (&v)[2][16], bool has2, float (&mb)[2], float (&den)[2]) {
  float s[2], q[2], x0[2];
#pragma unroll
  for (int r = 0; r < 2; ++r) {
    x0[r] = __builtin_bit_cast(float, __builtin_amdgcn_readfirstlane(__builtin_bit_cast(int, v[r][0])));
    float a = 0.f, b = 0.f;
#pragma unroll
    for (int i = 0; i < 8; ++i) { const float d = v[r][i] - x0[r]; a += d; b += d * d; }
    float a2 = 0.f, b2 = 0.f;
#pragma unroll
    for (int i = 8; i < 16; ++i) { const float d = v[r][i] - x0[r]; a2 += d; b2 += d * d; }
    s[r] = has2 ? a + a2 : a; q[r] = has2 ? b + b2 : b;
  }
#pragma unroll
  for (int r = 0; r < 2; ++r) { s[r] = wave_total(s[r]); q[r] = wave_total(q[r]); }
#pragma unroll
  for (int r = 0; r < 2; ++r) {
    const float md = s[r] * (1.0f / D_);
    const float var = rbf(fmaxf(q[r] * (1.0f / D_) - md * md, 0.f));
    mb[r] = rbf(x0[r] + md);
    den[r] = rbf(sqrtf(rbf(var + 1e-5f)));
  }
}

DI int w2_pos(int k) { return (k & ~12) | ((k & 4) << 1) | ((k & 8) >> 1); }
DI void tag_issue(u64& e, const u64* ctrl) { asm volatile("s_load_dwordx2 %0, %1, 0x0" : "=s"(e) : "s"(ctrl)); }
DI void tag_wait(u64& e) { asm volatile("s_waitcnt lgkmcnt(0)" : "+s"(e)); e += 1ull; }

struct StatRows { u32x4 p0[2], p1[2]; };
DI void stats_load(const u16* __restrict__ x, int row0, StatRows& R) {
  const int lane = threadIdx.x & 63;
#pragma unroll
  for (int r = 0; r < 2; ++r) {
    const u16* xr = x + (size_t)(row0 + r) * D_;
    asm volatile("global_load_dwordx4 %0, %1, off" : "=v"(R.p0[r]) : "v"(xr + lane * 8));
    asm volatile("global_load_dwordx4 %0, %1, off" : "=v"(R.p1[r]) : "v"(xr + 512 + (lane & 7) * 8));
  }
}
template <int N = 0> DI void stats_wait(StatRows& R) { asm volatile("s_waitcnt vmcnt(%4)" : "+v"(R.p0[0]), "+v"(R.p0[1]), "+v"(R.p1[0]), "+v"(R.p1[1]) : "n"(N)); }
DI void stats_compute(const StatRows& R, float* st, int row0) {
  const int lane = threadIdx.x & 63;
  const bool has2 = lane < 8;
  const u32x4 (&p0)[2] = R.p0; const u32x4 (&p1)[2] = R.p1;
  float v[2][16];
#pragma unroll
  for (int r = 0; r < 2; ++r)
#pragma unroll
    for (int i = 0; i < 4; ++i) { v[r][2 * i] = lo_f(p0[r][i]); v[r][2 * i + 1] = hi_f(p0[r][i]); v[r][8 + 2 * i] = lo_f(p1[r][i]); v[r][9 + 2 * i] = hi_f(p1[r][i]); }
  float mb1[2], den1[2], mb2[2], den2[2];
  ln_stats(v, has2, mb1, den1);
#pragma unroll
  for (int r = 0; r < 2; ++r) {
    const float inv = 1.0f / den1[r];
#pragma unroll
    for (int i = 0; i < 16; ++i) v[r][i] = (v[r][i] - mb1[r]) * inv;
  }
  ln_stats(v, has2, mb2, den2);
  if (lane < 4) {
    const int r = lane & 1, w = lane >> 1;
    const float m1 = r ? mb1[1] : mb1[0], d1 = r ? den1[1] : den1[0], m2 = r ? mb2[1] : mb2[0], d2 = r ? den2[1] : den2[0];
    const float rr = w ? 1.0f / (d1 * d2) : 1.0f / d1;
    const float cc = w ? m1 + m2 * d1 : m1;
    const u64 pk = ((u64)__float_as_uint(rr * cc) << 32) | __float_as_uint(rr);
    __hip_atomic_store((gu64*)st + w * S_ + row0 + r, pk, RLX_AGENT);
  }
}
template <int PER> DI void wait_flags(const u64* flags, int n, u64 tag) {
  const int lane = threadIdx.x & 63;
  for (unsigned spins = 0; spins < (1u << 21); ++spins) {
    bool ok = true;
#pragma unroll
    for (int i = 0; i < PER; ++i) {
      const int idx = i * 64 + lane;
      const u64 v = __hip_atomic_load((gu64*)flags + (idx < n ? idx : 0), RLX_AGENT);
      ok = ok && (v == tag || idx >= n);
    }
    if (__all(ok)) break;
    __builtin_amdgcn_s_sleep(1);
  }
}

template <int I, int N, class F> DI void static_for(F&& f) {
  if constexpr (I < N) { f(std::integral_constant<int, I>{}); static_for<I + 1, N>(f); }
}
template <int OFF> DI void ds_rd128(bf16x8& d, unsigned addr) {
  asm volatile("ds_read_b128 %0, %1 offset:%2" : "=v"(d) : "v"(addr), "i"(OFF));
}
template <int OFF> DI void ds_rdtr(s16x4& d, unsigned addr) {
  asm volatile("ds_read_b64_tr_b16 %0, %1 offset:%2" : "=v"(d) : "v"(addr), "i"(OFF));
}
DI void glds16(unsigned lds, unsigned voff, const char* sbase) {
  asm volatile("s_mov_b32 m0, %0\n\ts_nop 0\n\tglobal_load_lds_dwordx4 %1, %2" ::"s"(lds), "v"(voff), "s"(sbase) : "memory");
}
template <int LOFF, int GOFF> DI void glds16i(unsigned lds, unsigned voff, unsigned blo, unsigned bhi) {
  asm volatile("s_add_u32 vcc_lo, %2, %5\n\ts_addc_u32 vcc_hi, %3, 0\n\ts_add_u32 m0, %0, %4\n\ts_nop 0\n\tglobal_load_lds_dwordx4 %1, vcc"
               ::"s"(lds), "v"(voff), "s"(blo), "s"(bhi), "i"(LOFF), "i"(GOFF) : "memory", "vcc", "scc");
}
DI void glds16c(unsigned lds, unsigned voff, const char* sbase) {
  asm volatile("s_mov_b32 m0, %0\n\ts_nop 0\n\tglobal_load_lds_dwordx4 %1, %2 sc1" ::"s"(lds), "v"(voff), "s"(sbase) : "memory");
}
DI void glds16c_half(unsigned lds, unsigned voff, const char* sbase) {
  asm volatile("s_mov_b32 m0, %0\n\ts_mov_b32 exec_hi, 0\n\tglobal_load_lds_dwordx4 %1, %2 sc1\n\ts_mov_b32 exec_hi, -1" ::"s"(lds), "v"(voff), "s"(sbase) : "memory");
}
DI void glds16_half(unsigned lds, unsigned voff, const char* sbase) {
  asm volatile("s_mov_b32 m0, %0\n\ts_mov_b32 exec_hi, 0\n\tglobal_load_lds_dwordx4 %1, %2\n\ts_mov_b32 exec_hi, -1" ::"s"(lds), "v"(voff), "s"(sbase) : "memory");
}
DI void mfma_acc(f32x4& acc, const bf16x8& a, const bf16x8& b) {
  asm volatile("v_mfma_f32_16x16x32_bf16 %0, %1, %2, %0" : "+v"(acc) : "v"(a), "v"(b));
}
DI void mfma_new(f32x4& acc, const bf16x8& a, const bf16x8& b) {
  asm volatile("v_mfma_f32_16x16x32_bf16 %0, %1, %2, 0" : "+v"(acc) : "v"(a), "v"(b));
}
DI void mfma_settle() { asm volatile("s_nop 7\n\ts_nop 7" ::: "memory"); }
template <class T> DI void tie(T& v) { asm volatile("" : "+v"(v)); }

DI f32x2 gelu2(f32x2 x) {
  const float c0 = -2.0f * 0.7978845608028654f * 1.4426950408889634f;
  const float c1 = c0 * 0.044715f;
  const f32x2 z = x * (x * x * c1 + c0);
  f32x2 d;
  d.x = __builtin_amdgcn_exp2f(z.x); d.y = __builtin_amdgcn_exp2f(z.y);
  d += 1.0f;
  f32x2 r;
  r.x = __builtin_amdgcn_rcpf(d.x); r.y = __builtin_amdgcn_rcpf(d.y);
  return x * r;
}

constexpr int AUX0 = 2;
constexpr int HAUX = 16;
constexpr unsigned K2_EPI = 4 * 8192;
constexpr int K2_NCH = 20, K2_NK = 9, K2_TOT = 7 * K2_NK;
DI int nn_u(int k) { return ((k >> 1) & 1) | (((k >> 3) & 1) << 1); }
DI unsigned vadd_s(unsigned v, unsigned sc) { unsigned r; asm volatile("v_add_u32 %0, %1, %2" : "=v"(r) : "s"(sc), "v"(v)); return r; }
DI unsigned vxad_s(unsigned v, unsigned sc) { unsigned r; asm volatile("v_xad_u32 %0, %1, 64, %2" : "=v"(r) : "v"(v), "s"(sc)); return r; }
DI void flag_issue(u64& v, const u64* p) { asm volatile("global_load_dwordx2 %0, %1, off sc1" : "=v"(v) : "v"(p)); }
DI void flag_wait(u64& v) { asm volatile("s_waitcnt vmcnt(0)" : "+v"(v)); }
DI void touch1(unsigned& sink, const char* p) { asm volatile("global_load_dword %0, %1, off" : "+v"(sink) : "v"(p)); }
struct K2Aux { const char* ws_re; const char* ws_im; const char* w2; const u16* x; float* st; u64* flags0; const u64* ctrl; const char* e_st; const char* e_bias; int blk; int pm; const char* outp; };

template <bool BNN, bool SWAP, int ROLE>
DI void k2_main(unsigned lds0, int wave, const char* gA, const char* gB, int ldb_b, const K2Aux& aux, f32x4 (&acc)[4][6], u64& tag) {
  const int lane = threadIdx.x & 63, fr = lane & 15, fq = lane >> 4;
  const int wm = wave >> 1, wn = wave & 1;
  unsigned vA[2], vB[2];
  if (ROLE == 0) {
#pragma unroll
    for (int i = 0; i < 2; ++i) {
      const int bb = (wave * 2 + i) * 1024 + lane * 16, r = bb >> 7, c = (bb >> 4) & 7;
      vA[i] = (unsigned)(r * (D_ * 2) + ((c ^ ((r >> 1) & 7)) * 16));
      if (!BNN) vB[i] = (unsigned)(r * ldb_b + ((c ^ ((r >> 1) & 7)) * 16));
      else vB[i] = (unsigned)(r * ldb_b + ((c ^ (2 * nn_u(r))) * 16));
    }
  }
  const unsigned ldsw = lds0 + (unsigned)(wave * 2048);
  const unsigned gAlo = (unsigned)(size_t)gA, gAhi = (unsigned)((size_t)gA >> 32), gBlo = (unsigned)(size_t)gB, gBhi = (unsigned)((size_t)gB >> 32);
  auto issue = [&](auto gc, auto ic) {
    constexpr int G = decltype(gc)::value, I = decltype(ic)::value, ST = G / 7, C = G % 7, SLOT = G % K2_NCH;
    if constexpr (ROLE == 0 && G < K2_TOT) {
      if constexpr (C < 4) glds16i<SLOT * 8192 + I * 1024, C * 64 * D_ * 2 + ST * 128>(ldsw, vA[I], gAlo, gAhi);
      else if constexpr (!BNN) glds16i<SLOT * 8192 + I * 1024, (C - 4) * 64 * (D_ * 2) + ST * 128>(ldsw, vB[I], gBlo, gBhi);
      else glds16i<SLOT * 8192 + I * 1024, ST * 64 * (F_ * 2) + (C - 4) * 128>(ldsw, vB[I], gBlo, gBhi);
    }
  };
  unsigned sink = 0;
  const int t4 = threadIdx.x - 256;
  auto aux_step = [&](auto sc) {
    constexpr int STEP = decltype(sc)::value;
    if constexpr (ROLE == 1 && STEP < 12) {
      if (wave == 4 + (STEP % 3)) {
        const int li = (STEP * 240 + aux.blk) * 64 + lane;
        if (li < 131072 + 41472) {
          const char* p = li < 65536 ? aux.ws_re + (size_t)li * 128 : (li < 131072 ? aux.ws_im + (size_t)(li - 65536) * 128 : aux.w2 + (size_t)(li - 131072) * 128);
          touch1(sink, p);
        }
      }
    }
  };
  unsigned aoff[2], boff[6];
#pragma unroll
  for (int kk = 0; kk < 2; ++kk) aoff[kk] = lds0 + (unsigned)(fr * 128 + (((kk * 4 + fq) ^ ((fr >> 1) & 7)) * 16));
#pragma unroll
  for (int ni = 0; ni < 6; ++ni) {
    const int f4 = (wn * 6 + (ni ^ (wm & 1))) & 3;
    if (!BNN) boff[ni] = lds0 + (unsigned)((f4 * 16 + fr) * 128 + ((fq ^ ((fr >> 1) & 7)) * 16));
    else {
      const int g = lane >> 4, q = (lane & 15) >> 2, p = lane & 3, k = 8 * g + q;
      boff[ni] = lds0 + (unsigned)(k * 128 + ((2 * (f4 ^ nn_u(k)) + (p >> 1)) * 16) + (p & 1) * 8);
    }
  }
  struct Fr { bf16x8 a[4]; bf16x8 b[6]; s16x4 bl[6], bh[6]; };
  Fr f0, f1;
  constexpr int NRD = 4 + (BNN ? 12 : 6);
  unsigned ra, rb[6];
  auto set_addr = [&](auto sc, auto kc) {
    constexpr int ST = decltype(sc)::value, KK = decltype(kc)::value;
    int sa = (7 * ST) % K2_NCH + wm; if (sa >= K2_NCH) sa -= K2_NCH;
    ra = vadd_s(aoff[KK], (unsigned)(sa * 8192));
    constexpr unsigned S0 = ((7 * ST + 4) % K2_NCH) * 8192, S1 = ((7 * ST + 5) % K2_NCH) * 8192, S2 = ((7 * ST + 6) % K2_NCH) * 8192;
    const unsigned lo = wn ? S1 : S0, hi = wn ? S2 : S1, mid = wn ? S2 : S0;
#pragma unroll
    for (int ni = 0; ni < 6; ++ni) {
      const unsigned sb = ni < 2 ? lo : (ni < 4 ? mid : hi);
      if (!BNN && KK == 1) rb[ni] = vxad_s(boff[ni], sb); else rb[ni] = vadd_s(boff[ni], sb);
    }
  };
  auto rd = [&](auto kc, auto jc, Fr& f) {
    constexpr int KK = decltype(kc)::value, J = decltype(jc)::value;
    if constexpr (J < 4) ds_rd128<J * 2048>(f.a[J], ra);
    else if constexpr (!BNN) ds_rd128<0>(f.b[J - 4], rb[J - 4]);
    else {
      constexpr int ni = (J - 4) >> 1, t = (J - 4) & 1;
      if constexpr (t == 0) ds_rdtr<KK * 4096>(f.bl[ni], rb[ni]); else ds_rdtr<KK * 4096 + 512>(f.bh[ni], rb[ni]);
    }
  };
  auto waitf = [&](Fr& f) {
    asm volatile("s_waitcnt lgkmcnt(0)" ::: "memory");
#pragma unroll
    for (int i = 0; i < 4; ++i) tie(f.a[i]);
#pragma unroll
    for (int i = 0; i < 6; ++i) { if (!BNN) tie(f.b[i]); else { tie(f.bl[i]); tie(f.bh[i]); } }
  };
  auto mm = [&](auto ic, const Fr& f) {
    constexpr int I = decltype(ic)::value, mi = I / 6, ni = I % 6;
    bf16x8 bb;
    if constexpr (!BNN) bb = f.b[ni]; else bb = __builtin_shufflevector(f.bl[ni], f.bh[ni], 0, 1, 2, 3, 4, 5, 6, 7);
    if (SWAP) mfma_acc(acc[mi][ni], bb, f.a[mi]); else mfma_acc(acc[mi][ni], f.a[mi], bb);
  };
  f32x4 csa[3] = {(f32x4){0.f, 0.f, 0.f, 0.f}, (f32x4){0.f, 0.f, 0.f, 0.f}, (f32x4){0.f, 0.f, 0.f, 0.f}};
  bf16x8 ones = {0x3f80, 0x3f80, 0x3f80, 0x3f80, 0x3f80, 0x3f80, 0x3f80, 0x3f80};
  auto mcs = [&](auto jc, const Fr& f) {
    constexpr int J = decltype(jc)::value, ni = 2 * J;
    bf16x8 bb;
    if constexpr (!BNN) bb = f.b[ni]; else bb = __builtin_shufflevector(f.bl[ni], f.bh[ni], 0, 1, 2, 3, 4, 5, 6, 7);
    if (SWAP) mfma_acc(csa[J], bb, ones); else mfma_acc(csa[J], ones, bb);
  };
  auto substep = [&](Fr& fc, Fr& fn, auto rsc, auto rkc, auto p0c, auto p1c, auto ckc) {
    constexpr int RS = decltype(rsc)::value, RK = decltype(rkc)::value, P0 = decltype(p0c)::value, P1 = decltype(p1c)::value;
    if constexpr (RS >= 0) set_addr(std::integral_constant<int, (RS >= 0 ? RS : 0)>{}, rkc);
    sched_fence();
    static_for<0, 24>([&](auto ic) {
      constexpr int I = decltype(ic)::value;
      mm(ic, fc);
      if constexpr (RS >= 0 && I < NRD) rd(rkc, ic, fn);
      constexpr int NP = P1 - P0;
      if constexpr (I >= 24 - 2 * NP && ((24 - I) % 2) == 0) {
        constexpr int P = P1 - (24 - I) / 2;
        issue(std::integral_constant<int, P / 2>{}, std::integral_constant<int, P % 2>{});
      }
      sched_fence();
    });
    if constexpr (decltype(ckc)::value == ROLE) static_for<0, 3>([&](auto jc) { mcs(jc, fc); sched_fence(); });
  };
  using IC = std::integral_constant<int, 0>;
  (void)sizeof(IC);
  StatRows srows;
  if constexpr (ROLE == 0) {
    static_for<0, 14>([&](auto gc) { issue(gc, std::integral_constant<int, 0>{}); issue(gc, std::integral_constant<int, 1>{}); });
    tag_issue(tag, aux.ctrl); tag_wait(tag);
    wait_vm<14>();
  } else {
    stats_load(aux.x, aux.blk * 8 + (wave - 4) * 2, srows);
    {
      const int li = ((aux.blk >> 3) * 4 + (wave - 4)) * 64 + lane;
      constexpr int G = 65536;
      constexpr int n1 = S_ * S_ * 2 / G, n2 = D_ * F_ * 2 / G, n3 = S_ * D_ * 2 / G;
      const char* p = (const char*)aux.x;
      if (li < n1) p = aux.ws_re + (size_t)li * G;
      else if (li < 2 * n1) p = aux.ws_im + (size_t)(li - n1) * G;
      else if (li < 2 * n1 + n2) p = aux.w2 + (size_t)(li - 2 * n1) * G;
      else if (li < 2 * n1 + n2 + n3) p = aux.outp + (size_t)(li - 2 * n1 - n2) * G;
      touch1(sink, p);
    }
    tag_issue(tag, aux.ctrl);
    stats_wait<1>(srows); stats_compute(srows, aux.st, aux.blk * 8 + (wave - 4) * 2);
    tag_wait(tag);
  }
#pragma unroll
  for (int i = 0; i < 4; ++i)
#pragma unroll
    for (int j = 0; j < 6; ++j) { acc[i][j] = (f32x4){0.f, 0.f, 0.f, 0.f}; tie(acc[i][j]); }
  tie(ones); tie(csa[0]); tie(csa[1]); tie(csa[2]);
  raw_barrier();

  u64 fl = 0;
  set_addr(std::integral_constant<int, 0>{}, std::integral_constant<int, 0>{});
  static_for<0, NRD>([&](auto jc) { rd(std::integral_constant<int, 0>{}, jc, f0); });
  static_for<0, K2_NK>([&](auto sc) {
    constexpr int K = decltype(sc)::value;
    constexpr int PB = 2 * (7 * (K - 1) + K2_NCH);
    waitf(f0);
    if constexpr (K > AUX0) aux_step(std::integral_constant<int, 2 * (K - AUX0) - 1>{});
    if constexpr (K >= 1) substep(f0, f1, sc, std::integral_constant<int, 1>{}, std::integral_constant<int, PB + 7>{}, std::integral_constant<int, PB + 14>{}, std::integral_constant<int, 0>{});
    else substep(f0, f1, sc, std::integral_constant<int, 1>{}, std::integral_constant<int, 28>{}, std::integral_constant<int, 40>{}, std::integral_constant<int, 0>{});
    waitf(f1);
    if constexpr (K < K2_NK - 1) {
      constexpr int ISS = (7 * K + K2_NCH - 1 < K2_TOT - 1) ? 7 * K + K2_NCH - 1 : K2_TOT - 1;
      if (ROLE == 0) wait_vm<(ISS - (7 * K + 13)) * 2>();
      if constexpr (ROLE == 1 && K == 2) wait_vm<0>();
      if constexpr (ROLE == 1 && K == 7) { if (wave == 7) wait_vm<0>(); }
      __builtin_amdgcn_s_barrier();
      asm volatile("" ::: "memory");
      if constexpr (ROLE == 1 && K == 2) { if (threadIdx.x == 256) __hip_atomic_store((gu64*)aux.flags0 + aux.blk, tag, RLX_AGENT); }
      if constexpr (ROLE == 1 && K == 5) {
        if (wave == 7) flag_issue(fl, aux.flags0 + aux.pm * 32 + (lane & 31));
      }
      if constexpr (ROLE == 1 && K == 6) {
        if (wave == 7) {
          flag_wait(fl);
          if (!__all(fl == tag)) wait_flags<1>(aux.flags0 + aux.pm * 32, 32, tag);
          const unsigned eo = lds0 + K2_EPI;
          glds16c(eo, (unsigned)(lane * 16), aux.e_st);
          glds16c(eo + 1024u, (unsigned)(1024 + lane * 16), aux.e_st);
          glds16(eo + 3072u, (unsigned)((lane < 24 ? lane : 0) * 16), aux.e_bias);
        }
      }
      if constexpr (K >= AUX0) aux_step(std::integral_constant<int, 2 * (K - AUX0)>{});
      constexpr int PC = 2 * (7 * K + K2_NCH);
      substep(f1, f0, std::integral_constant<int, K + 1>{}, std::integral_constant<int, 0>{}, std::integral_constant<int, PC>{}, std::integral_constant<int, PC + 7>{}, std::integral_constant<int, 1>{});
    } else {
      substep(f1, f0, std::integral_constant<int, -1>{}, std::integral_constant<int, 0>{}, std::integral_constant<int, 0>{}, std::integral_constant<int, 0>{}, std::integral_constant<int, 1>{});
    }
  });
  mfma_settle();
  tie(csa[0]); tie(csa[1]); tie(csa[2]);
#pragma unroll
  for (int i = 0; i < 4; ++i)
#pragma unroll
    for (int j = 0; j < 6; ++j) tie(acc[i][j]);
  if (ROLE == 1) { asm volatile("s_waitcnt vmcnt(0)" : "+v"(sink)); }
  {
    LAS float* tb = (LAS float*)(size_t)(lds0 + K2_EPI + 4096u + ROLE * 768u);
#pragma unroll
    for (int jj = 0; jj < 3; ++jj) {
      const int c0 = wn * 96 + ((2 * jj) ^ (wm & 1)) * 16;
      if (SWAP) { if (fr == 0) *(LAS f32x4*)(tb + c0 + 4 * fq) = csa[jj]; }
      else { if (fq == 0) tb[c0 + fr] = csa[jj][0]; }
    }
  }
  raw_barrier();
}

constexpr int FL_G = 5;
constexpr int K3_NS = 4, K3_A = 16384, K3_B = 18432, K3_STAGE = K3_A + K3_B, K3_CNT = 9, K3_S = 16, K3_O = 18, K3_N = K3_S + K3_O;
constexpr unsigned K3_SP = 2 * K3_STAGE;
constexpr unsigned K3_SOWN = 4 * K3_STAGE;

struct K3Ctx {
  const char* sA; const char* sB; const char* oA; const char* oB;
  char* sp;
  u64* ctrl; u64* flagsA; u64* flags1; u64* flagsS;
  int blk, tile, q, rs, ro;
};

DI void k3_loader(unsigned lds0, int lw, const K3Ctx& c, u64 tag) {
  int tz = threadIdx.x; tie(tz);
  const int lane = tz & 63;
  constexpr int NS = K3_NS, STAGE = K3_STAGE, CNT = K3_CNT;
  unsigned vAs[4], vAo[4], vBs[5], vBo[5];
#pragma unroll
  for (int i = 0; i < 4; ++i) {
    const int b = (lw * 4 + i) * 1024 + lane * 16, r = b >> 7, cc = (b >> 4) & 7;
    vAs[i] = (unsigned)(r * (S_ * 2) + ((cc ^ ((r >> 1) & 7)) * 16));
    vAo[i] = (unsigned)(r * (F_ * 2) + ((cc ^ ((r >> 1) & 7)) * 16));
  }
#pragma unroll
  for (int i = 0; i < 5; ++i) {
    const int b = lw * 4608 + i * 1024 + ((i < 4) ? lane : (lane & 31)) * 16, r = b >> 7, cc = (b >> 4) & 7;
    vBs[i] = (unsigned)(r * (S_ * 2) + ((cc ^ ((r >> 1) & 7)) * 16));
    vBo[i] = (unsigned)(w2_pos(b / 288) * (D_ * 2) + (b % 288));
  }
  const unsigned la = lds0 + (unsigned)(lw * 4096), lb = lds0 + (unsigned)(K3_A + lw * 4608);
  auto stage_s = [&](int g, bool doA, bool doB) {
    const unsigned slot = (unsigned)((g & 3) * STAGE);
    const int kt = (g + c.rs) & 15;
    if (doA) {
#pragma unroll
      for (int i = 0; i < 4; ++i) glds16(la + slot + (unsigned)(i * 1024), vAs[i], c.sA + kt * 128);
    }
    if (doB) {
#pragma unroll
      for (int i = 0; i < 4; ++i) glds16c(lb + slot + (unsigned)(i * 1024), vBs[i], c.sB + kt * 128);
      glds16c_half(lb + slot + 4096u, vBs[4], c.sB + kt * 128);
    }
  };
  auto stage_o = [&](int g) {
    const unsigned slot = (unsigned)((g & 3) * STAGE);
    int kt = g - K3_S + c.ro; if (kt >= K3_O) kt -= K3_O;
#pragma unroll
    for (int i = 0; i < 4; ++i) glds16c(la + slot + (unsigned)(i * 1024), vAo[i], c.oA + kt * 128);
#pragma unroll
    for (int i = 0; i < 4; ++i) glds16(lb + slot + (unsigned)(i * 1024), vBo[i], c.oB + (long)kt * (64 * D_ * 2));
    glds16_half(lb + slot + 4096u, vBo[4], c.oB + (long)kt * (64 * D_ * 2));
  };
#pragma unroll
  for (int g = 0; g < NS; ++g) stage_s(g, true, false);
  if (lw == 0) wait_flags<1>(c.flagsA, 48, tag);

  raw_barrier();
#pragma unroll
  for (int g = 0; g < NS; ++g) stage_s(g, false, true);
  wait_vm<15>();
  raw_barrier();
  wait_vm<10>(); raw_barrier(); stage_s(4, true, true);
  wait_vm<5 + CNT>(); raw_barrier(); stage_s(5, true, true);
  for (int k = 2; k < K3_N - NS; ++k) {
    wait_vm<2 * CNT>();
    if (k == K3_S - NS && lw == 0) {

      wait_flags<4>(c.flags1, 256, tag);

      if (c.blk == 0 && lane == 0) __hip_atomic_store((gu64*)c.ctrl, tag, RLX_AGENT);
    }
    raw_barrier();
    if (k + NS < K3_S) stage_s(k + NS, true, true); else stage_o(k + NS);
    if (k == FL_G && tz == 256) { __hip_atomic_store((gu64*)c.flags1 + c.blk, tag, RLX_AGENT); }
    if (k == K3_S + 3 && tz == 256) __hip_atomic_store((gu64*)c.flagsS + c.tile * 4 + c.q, tag, RLX_AGENT);
  }
  const unsigned vl = (unsigned)(lane * 16);
  wait_vm<2 * CNT>();
  if (lw == 0) wait_flags<1>(c.flagsS + c.tile * 4, 4, tag);
  raw_barrier();
  wait_vm<CNT>();
  if (lw < 3) {
    const char* spsrc = c.sp + (size_t)((c.tile * 4 + c.q) * 4 + ((c.q + 1 + lw) & 3)) * 9216;
#pragma unroll
    for (int i = 0; i < 9; ++i) glds16c(lds0 + K3_SP + (unsigned)(lw * 9216 + i * 1024), vl, spsrc + i * 1024);
    raw_barrier();
    wait_vm<9>();
  } else {
    raw_barrier();
    wait_vm<0>();
  }
  raw_barrier();
  wait_vm<0>();
}

DI void k3_compute(unsigned lds0, int wave, const K3Ctx& c, f32x4 (&acc)[2][9]) {
  int tz = threadIdx.x; tie(tz);
  const int lane = tz & 63;
  constexpr int NS = K3_NS, STAGE = K3_STAGE;
  const int fr = lane & 15, fq = lane >> 4;
  const unsigned aoff = lds0 + (unsigned)((wave * 32 + fr) * 128 + ((fq ^ ((fr >> 1) & 7)) * 16));
  const unsigned boffT = lds0 + (unsigned)(K3_A + fr * 128 + ((fq ^ ((fr >> 1) & 7)) * 16));
  unsigned boffN;
  {
    const int g = lane >> 4, q = (lane & 15) >> 2, p = lane & 3;
    boffN = lds0 + (unsigned)(K3_A + ((g >> 1) * 16 + (g & 1) * 4 + q) * 288 + (p >> 1) * 16 + (p & 1) * 8);
  }
  const auto srs = __builtin_amdgcn_make_buffer_rsrc(c.sp, 0, 64 * 16 * 9216, 0x00020000);
  const unsigned vl = (unsigned)(lane * 16);
  const unsigned vsp = vl + (unsigned)(((c.tile * 4 + wave) * 4 + c.q) * 9216);
  struct Fr { bf16x8 a[2]; bf16x8 b[9]; s16x4 bl[9], bh[9]; };
  Fr f0, f1;
  auto rd = [&](auto nn, auto kc, auto jc, Fr& f, unsigned ad, unsigned bd) {
    constexpr int J = decltype(jc)::value, KK = decltype(kc)::value;
    if constexpr (J < 2) ds_rd128<J * 2048>(f.a[J], ad);
    else if constexpr (!decltype(nn)::value) ds_rd128<(J - 2) * 2048>(f.b[J - 2], bd);
    else { ds_rdtr<KK * 32 * 288 + (J - 2) * 32>(f.bl[J - 2], bd); ds_rdtr<KK * 32 * 288 + (J - 2) * 32 + 8 * 288>(f.bh[J - 2], bd); }
  };
  auto waitf = [&](auto nn, Fr& f) {
    asm volatile("s_waitcnt lgkmcnt(0)" ::: "memory");
    tie(f.a[0]); tie(f.a[1]);
#pragma unroll
    for (int i = 0; i < 9; ++i) { if (!decltype(nn)::value) tie(f.b[i]); else { tie(f.bl[i]); tie(f.bh[i]); } }
  };
  auto mm = [&](auto nn, auto pub, auto ic, const Fr& f) {
    constexpr int I = decltype(ic)::value, ni = I / 2, mi = I % 2;
    bf16x8 bb;
    if constexpr (!decltype(nn)::value) bb = f.b[ni]; else bb = __builtin_shufflevector(f.bl[ni], f.bh[ni], 0, 1, 2, 3, 4, 5, 6, 7);
    if constexpr (decltype(pub)::value) {
      if constexpr (mi == 0) {
        if (wave == c.q) {
          *(LAS f32x4*)(size_t)(lds0 + K3_SOWN + ni * 1024 + vl) = acc[0][ni];
          *(LAS f32x4*)(size_t)(lds0 + K3_SOWN + (9 + ni) * 1024 + vl) = acc[1][ni];
        } else {
          const u32x4 w = {pk_bf16(acc[0][ni][0], acc[0][ni][1]), pk_bf16(acc[0][ni][2], acc[0][ni][3]),
                           pk_bf16(acc[1][ni][0], acc[1][ni][1]), pk_bf16(acc[1][ni][2], acc[1][ni][3])};
          __builtin_amdgcn_raw_buffer_store_b128(w, srs, vsp + ni * 1024, 0, 16);
        }
        asm volatile("s_nop 1" ::: "memory");
      }
      mfma_new(acc[mi][ni], bb, f.a[mi]);
    } else mfma_acc(acc[mi][ni], bb, f.a[mi]);
  };
  auto sub = [&](auto cn, auto pub, Fr& fc, Fr& fn, auto rnc, auto rkc, unsigned rslot) {
    constexpr int RN = decltype(rnc)::value, RK = decltype(rkc)::value;
    using NXT = std::integral_constant<bool, RN == 1>;
    unsigned ad = 0, bd = 0;
    if constexpr (RN >= 0) {
      ad = RK ? vxad_s(aoff, rslot) : vadd_s(aoff, rslot);
      if constexpr (RN == 1) bd = vadd_s(boffN, rslot); else bd = RK ? vxad_s(boffT, rslot) : vadd_s(boffT, rslot);
    }
    static_for<0, 18>([&](auto ic) {
      constexpr int I = decltype(ic)::value;
      mm(cn, pub, ic, fc);
      if constexpr (RN >= 0 && I < 11) rd(NXT{}, rkc, ic, fn, ad, bd);
    });
  };
  using F = std::false_type; using T = std::true_type;
  using N0 = std::integral_constant<int, 0>; using N1 = std::integral_constant<int, 1>; using NL = std::integral_constant<int, -1>;
  auto body = [&](auto cn, auto nxc, auto pub, unsigned cslot, unsigned nslot, int ev) {
    constexpr int NX = decltype(nxc)::value;
    waitf(cn, f0);
    sub(cn, pub, f0, f1, std::integral_constant<int, decltype(cn)::value ? 1 : 0>{}, N1{}, cslot);
    waitf(cn, f1);
    if (ev) wait_vm<0>();
    if constexpr (NX >= 0) { __builtin_amdgcn_s_barrier(); asm volatile("" ::: "memory"); }
    sub(cn, F{}, f1, f0, nxc, N0{}, nslot);
  };
#pragma unroll
  for (int i = 0; i < 2; ++i)
#pragma unroll
    for (int jj = 0; jj < 9; ++jj) { acc[i][jj] = (f32x4){0.f, 0.f, 0.f, 0.f}; tie(acc[i][jj]); }
  raw_barrier();
  raw_barrier();
  static_for<0, 11>([&](auto jc) { rd(F{}, N0{}, jc, f0, aoff, boffT); });
  unsigned cslot = 0, nslot = STAGE;
  auto adv = [&]() { cslot = nslot; nslot += STAGE; if (nslot == NS * STAGE) nslot = 0; };
  for (int g = 0; g < K3_S - 1; ++g) { body(F{}, N0{}, F{}, cslot, nslot, g == FL_G ? 1 : 0); adv(); }
  body(F{}, N1{}, F{}, cslot, nslot, 0); adv();
  mfma_settle();
  body(T{}, N1{}, T{}, cslot, nslot, 0); adv();
  for (int g = K3_S + 1; g < K3_N - 1; ++g) { body(T{}, N1{}, F{}, cslot, nslot, g == K3_S + 3 ? 1 : 0); adv(); }
  body(T{}, NL{}, F{}, cslot, 0u, 0);
  mfma_settle();
#pragma unroll
  for (int i = 0; i < 2; ++i)
#pragma unroll
    for (int jj = 0; jj < 9; ++jj) tie(acc[i][jj]);
}

constexpr size_t SD2 = (size_t)S_ * D_ * 2;
constexpr size_t WS_ST = 0, WS_CTRL = 65536, WS_ABT = 131072, WS_H = WS_ABT + 2 * SD2,
                 WS_SP = WS_H + (size_t)S_ * F_ * 2, WS_OP = WS_SP + (size_t)64 * 16 * 9216;

__global__ __launch_bounds__(512, 2) void k_all(const u16* __restrict__ x, const u16* __restrict__ ws_re, const u16* __restrict__ ws_im,
                                                const u16* __restrict__ wd_re, const u16* __restrict__ wd_im,
                                                const u16* __restrict__ w1, const u16* __restrict__ b1,
                                                const u16* __restrict__ w2, const u16* __restrict__ b2,
                                                u16* __restrict__ out, char* ws) {
  __shared__ __attribute__((aligned(1024))) char smem_[K2_NCH * 8192];
  LAS char* smem = (LAS char*)smem_;

  const int b = blockIdx.x, tid = threadIdx.x;
  const int lane = tid & 63, fr = lane & 15, fq = lane >> 4;
  const int wave = __builtin_amdgcn_readfirstlane(tid >> 6);
  const unsigned lds0 = (unsigned)(size_t)smem;
  float* st = (float*)(ws + WS_ST);
  u16* abT = (u16*)(ws + WS_ABT);
  u16* h = (u16*)(ws + WS_H);
  u64* ctrl = (u64*)(ws + WS_CTRL);
  u64* flags0 = ctrl + 512; u64* flagsA = ctrl + 1024; u64* flags1 = ctrl + 1536; u64* flagsS = ctrl + 2048; u64* flags2 = ctrl + 2560;
  u64 tag;

  if (b >= 240) {
    tag_issue(tag, ctrl);
    if (wave >= 4) { StatRows sr; stats_load(x, b * 8 + (wave - 4) * 2, sr); stats_wait(sr); stats_compute(sr, st, b * 8 + (wave - 4) * 2); }
    tag_wait(tag);
    wait_vm<0>();
    raw_barrier();
    if (tid == 256) __hip_atomic_store((gu64*)flags0 + b, tag, RLX_AGENT);
  } else {
    const int x8 = b & 7, j8 = b >> 3;
    const int u = j8 < 24 ? j8 : (x8 >> 1) * 6 + (j8 - 24);
    const int pm = (x8 & 1) * 4 + (u & 3), pn = j8 < 24 ? 6 + (x8 >> 1) * 6 + (u >> 2) : (u >> 2);
    const int t = pn * 8 + pm;
    const int wm = wave >> 1, wn = wave & 1, par = wm & 1;
    K2Aux aux{(const char*)ws_re, (const char*)ws_im, (const char*)w2, x, st, flags0, ctrl,
              (const char*)(st + ((size_t)(pn < 6 ? 0 : 1) * S_ + pm * 256) * 2),
              (const char*)(b1 + (pn < 6 ? 0 : (pn - 6) * 192)), b, pm, (const char*)out};
    const LAS char* epi = smem + K2_EPI;
    const char* gA = (const char*)(x + (size_t)pm * 256 * D_);
    f32x4 acc[4][6];
    if (pn < 6) {
      const u16* wd = pn < 3 ? wd_re : wd_im;
      const int n0 = (pn < 3 ? pn : pn - 3) * 192;
      const char* gB = (const char*)(wd + (size_t)n0 * D_);
      if (wave < 4) k2_main<false, false, 0>(lds0, wave, gA, gB, D_ * 2, aux, acc, tag);
      else k2_main<false, false, 1>(lds0, wave, gA, gB, D_ * 2, aux, acc, tag);

      u16* dst = abT + (size_t)(pn < 3 ? 0 : 1) * D_ * S_;
      const auto ars = __builtin_amdgcn_make_buffer_rsrc(dst, 0, D_ * S_ * 2, 0x00020000);
      float cv[6];
#pragma unroll
      for (int ni = 0; ni < 6; ++ni) {
        const int cl = wn * 96 + (ni ^ par) * 16 + fr;
        cv[ni] = *(const LAS float*)(epi + 4096 + cl * 4) + *(const LAS float*)(epi + 4096 + 768 + cl * 4);
      }
#pragma unroll
      for (int mi = 0; mi < 4; ++mi) {
        const f32x4 s01 = *(const LAS f32x4*)(epi + (wm * 64 + mi * 16 + 4 * fq) * 8), s23 = *(const LAS f32x4*)(epi + (wm * 64 + mi * 16 + 4 * fq) * 8 + 16);
        const f32x4 rr = {s01[0], s01[2], s23[0], s23[2]}, rc = {s01[1], s01[3], s23[1], s23[3]};
#pragma unroll
        for (int ni = 0; ni < 6; ++ni) acc[mi][ni] = acc[mi][ni] * rr - rc * cv[ni];
      }
#pragma unroll
      for (int ni = 0; ni < 6; ++ni) {
        const int e = n0 + wn * 96 + (ni ^ par) * 16 + fr;
#pragma unroll
        for (int mp = 0; mp < 2; ++mp) {
          unsigned x0 = pk_bf16(acc[2 * mp][ni][0], acc[2 * mp][ni][1]), x1 = pk_bf16(acc[2 * mp][ni][2], acc[2 * mp][ni][3]);
          unsigned y0 = pk_bf16(acc[2 * mp + 1][ni][0], acc[2 * mp + 1][ni][1]), y1 = pk_bf16(acc[2 * mp + 1][ni][2], acc[2 * mp + 1][ni][3]);
          auto r0 = __builtin_amdgcn_permlane16_swap(x0, y0, false, false);
          auto r1 = __builtin_amdgcn_permlane16_swap(x1, y1, false, false);
          u32x4 w = {r0[0], r1[0], r0[1], r1[1]};
          const int s = pm * 256 + wm * 64 + (2 * mp + (fq & 1)) * 16 + (fq >> 1) * 8;
          __builtin_amdgcn_raw_buffer_store_b128(w, ars, (e * S_ + s) * 2, 0, 16);
        }
      }
      wait_vm<0>();
      raw_barrier();
      if (tid == 0) __hip_atomic_store((gu64*)flagsA + t, tag, RLX_AGENT);

    } else {
      const int n0 = (pn - 6) * 192;
      const char* gB = (const char*)(w1 + n0);
      if (wave < 4) k2_main<true, true, 0>(lds0, wave, gA, gB, F_ * 2, aux, acc, tag);
      else k2_main<true, true, 1>(lds0, wave, gA, gB, F_ * 2, aux, acc, tag);

      const auto hrs = __builtin_amdgcn_make_buffer_rsrc(h, 0, F_ * S_ * 2, 0x00020000);
      float bias[6][4];
      f32x4 csv[6];
      u32x4 hw[4][3];
#pragma unroll
      for (int ni = 0; ni < 6; ++ni) {
        const int cl = wn * 96 + (ni ^ par) * 16 + 4 * fq;
        const u32x2 p = *(const LAS u32x2*)(epi + 3072 + cl * 2);
        bias[ni][0] = lo_f(p[0]); bias[ni][1] = hi_f(p[0]); bias[ni][2] = lo_f(p[1]); bias[ni][3] = hi_f(p[1]);
        csv[ni] = *(const LAS f32x4*)(epi + 4096 + cl * 4) + *(const LAS f32x4*)(epi + 4096 + 768 + cl * 4);
      }
#pragma unroll
      for (int mi = 0; mi < 4; ++mi) {
        const f32x2 sr = *(const LAS f32x2*)(epi + (wm * 64 + mi * 16 + fr) * 8);
#pragma unroll
        for (int np = 0; np < 3; ++np) {
          unsigned pk[2][2];
#pragma unroll
          for (int o = 0; o < 2; ++o) {
            const int ni = 2 * np + o;
            const f32x2 t01 = (f32x2){bias[ni][0], bias[ni][1]} - (f32x2){csv[ni][0], csv[ni][1]} * sr[1];
            const f32x2 t23 = (f32x2){bias[ni][2], bias[ni][3]} - (f32x2){csv[ni][2], csv[ni][3]} * sr[1];
            const f32x2 g01 = gelu2((f32x2){acc[mi][ni][0], acc[mi][ni][1]} * sr[0] + t01);
            const f32x2 g23 = gelu2((f32x2){acc[mi][ni][2], acc[mi][ni][3]} * sr[0] + t23);
            pk[o][0] = pk_bf16(g01.x, g01.y); pk[o][1] = pk_bf16(g23.x, g23.y);
          }
          auto r0 = __builtin_amdgcn_permlane16_swap(pk[0][0], pk[1][0], false, false);
          auto r1 = __builtin_amdgcn_permlane16_swap(pk[0][1], pk[1][1], false, false);
          hw[mi][np] = (u32x4){r0[0], r1[0], r0[1], r1[1]};
        }
      }
      sched_fence();
      sched_fence();
#pragma unroll
      for (int mi = 0; mi < 4; ++mi)
#pragma unroll
        for (int np = 0; np < 3; ++np) {
          const int m = pm * 256 + wm * 64 + mi * 16 + fr;
          const int f = n0 + wn * 96 + ((2 * np + (fq & 1)) ^ par) * 16 + (fq >> 1) * 8;
          __builtin_amdgcn_raw_buffer_store_b128(hw[mi][np], hrs, (m * F_ + f) * 2, 0, HAUX);
        }

    }
  }

  raw_barrier();

  const int xcd = b & 7, j3 = b >> 3;
  const int q = xcd >> 1, pm3 = (xcd & 1) * 8 + (j3 >> 2), pn3 = j3 & 3;
  const int tile = pm3 * 4 + pn3;
  K3Ctx c;
  c.sA = (const char*)((q < 2 ? ws_re : ws_im) + (size_t)pm3 * 128 * S_ + (q & 1) * 1024);
  c.sB = (const char*)(abT + (size_t)(q >> 1) * D_ * S_ + (size_t)pn3 * 144 * S_ + (q & 1) * 1024);
  c.oA = (const char*)(h + (size_t)pm3 * 128 * F_ + q * 1152);
  c.oB = (const char*)(w2 + (size_t)q * 1152 * D_ + pn3 * 144);
  c.sp = ws + WS_SP;
  c.ctrl = ctrl; c.flagsA = flagsA; c.flags1 = flags1; c.flagsS = flagsS;
  c.blk = b; c.tile = tile; c.q = q; c.rs = (xcd & 1) * 8; c.ro = (xcd & 1) * 9;
  f32x4 acc[2][9];

  if (wave >= 4) k3_loader(lds0, wave - 4, c, tag);
  else k3_compute(lds0, wave, c, acc);

  int tz = threadIdx.x; tie(tz);
  const int lz = tz & 63;
  const auto prs = __builtin_amdgcn_make_buffer_rsrc(ws + WS_OP, 0, 64 * 16 * 9216, 0x00020000);
  LAS f32x4* Y = (LAS f32x4*)(smem + 3 * K3_STAGE);
  if (wave < 4) {
    if (wave == q) {
#pragma unroll
      for (int i = 0; i < 18; ++i) Y[i * 64 + lz] = acc[i / 9][i % 9];
    } else {
#pragma unroll
      for (int i = 0; i < 9; ++i) {
        const u32x4 w = {pk_bf16(acc[0][i][0], acc[0][i][1]), pk_bf16(acc[0][i][2], acc[0][i][3]), pk_bf16(acc[1][i][0], acc[1][i][1]), pk_bf16(acc[1][i][2], acc[1][i][3])};
        __builtin_amdgcn_raw_buffer_store_b128(w, prs, lz * 16 + i * 1024 + ((tile * 4 + wave) * 4 + q) * 9216, 0, 16);
      }
    }
  }

  asm volatile("s_waitcnt vmcnt(0)" ::: "memory");
  raw_barrier();

  if (tz == 0) __hip_atomic_store((gu64*)flags2 + tile * 4 + q, tag, RLX_AGENT);
  u32x2 xb[3], bb[3];
  int ooff[3];
  float sv[3][4];
#pragma unroll
  for (int it = 0; it < 3; ++it) {
    int idx = tz + it * 512; if (idx >= 1152) idx = tz;
    const int fi = idx >> 6, l = idx & 63;
    const int mrow = pm3 * 128 + q * 32 + (fi / 9) * 16 + (l & 15);
    const int ncol = pn3 * 144 + (fi % 9) * 16 + 4 * (l >> 4);
    ooff[it] = mrow * D_ + ncol;
    if (wave != 7) { xb[it] = *(const u32x2*)(x + ooff[it]); bb[it] = *(const u32x2*)(b2 + ncol); }
    const f32x4 own = *(const LAS f32x4*)(smem + K3_SOWN + idx * 16);
    f32x4 re = q < 2 ? own : (f32x4){0.f, 0.f, 0.f, 0.f}, im = q < 2 ? (f32x4){0.f, 0.f, 0.f, 0.f} : own;
#pragma unroll
    for (int jj = 0; jj < 3; ++jj) {
      const u32x2 a = *(const LAS u32x2*)(smem + K3_SP + jj * 9216 + ((fi % 9) * 64 + l) * 16 + (fi / 9) * 8);
      const f32x4 v = {lo_f(a[0]), hi_f(a[0]), lo_f(a[1]), hi_f(a[1])};
      if (((q + 1 + jj) & 3) < 2) re += v; else im += v;
    }
#pragma unroll
    for (int i = 0; i < 4; ++i) sv[it][i] = rbf(rbf(re[i]) - rbf(im[i]));
  }
  if (wave == 7) {
    wait_flags<1>(flags2 + tile * 4, 4, tag);
#pragma unroll
    for (int it = 0; it < 3; ++it) { xb[it] = *(const u32x2*)(x + ooff[it]); bb[it] = *(const u32x2*)(b2 + ooff[it] % D_); }
  }
  raw_barrier();

  u32x2 p[3][3];
#pragma unroll
  for (int it = 0; it < 3; ++it) {
    int idx = tz + it * 512; if (idx >= 1152) idx = tz;
    const int fi = idx >> 6, l = idx & 63;
#pragma unroll
    for (int jj = 0; jj < 3; ++jj) {
      const int sw = (q + 1 + jj) & 3;
      p[it][jj] = __builtin_bit_cast(u32x2, __builtin_amdgcn_raw_buffer_load_b64(prs, ((fi % 9) * 64 + l) * 16 + (fi / 9) * 8, ((tile * 4 + q) * 4 + sw) * 9216, 16));
    }
  }
#pragma unroll
  for (int it = 0; it < 3; ++it) {
    const int idx = tz + it * 512;
    if (idx < 1152) {
      const f32x4 own = Y[idx];
      const float xv[4] = {lo_f(xb[it][0]), hi_f(xb[it][0]), lo_f(xb[it][1]), hi_f(xb[it][1])};
      const float bv[4] = {lo_f(bb[it][0]), hi_f(bb[it][0]), lo_f(bb[it][1]), hi_f(bb[it][1])};
      float ov[4] = {own[0], own[1], own[2], own[3]};
#pragma unroll
      for (int jj = 0; jj < 3; ++jj) { ov[0] += lo_f(p[it][jj][0]); ov[1] += hi_f(p[it][jj][0]); ov[2] += lo_f(p[it][jj][1]); ov[3] += hi_f(p[it][jj][1]); }
      float o[4];
#pragma unroll
      for (int i = 0; i < 4; ++i) {
        const float z = rbf(rbf(ov[i]) + bv[i]);
        const float g = rbf(z * __builtin_amdgcn_rcpf(1.0f + __builtin_amdgcn_exp2f(-1.4426950408889634f * z)));
        o[i] = xv[i] + rbf(sv[it][i] * g);
      }
      u32x2 w = {pk_bf16(o[0], o[1]), pk_bf16(o[2], o[3])};
      *(u32x2*)(out + ooff[it]) = w;
    }
  }

}

extern "C" void kernel_launch(void* const* d_in, const int* in_sizes, int n_in, void* d_out, int out_size, void* d_ws,
                              size_t ws_size, hipStream_t stream) {
  k_all<<<256, 512, 0, stream>>>((const u16*)d_in[0], (const u16*)d_in[1], (const u16*)d_in[2], (const u16*)d_in[3], (const u16*)d_in[4],
                                 (const u16*)d_in[6], (const u16*)d_in[7], (const u16*)d_in[8], (const u16*)d_in[9], (u16*)d_out, (char*)d_ws);
}
```
